```python
import torch

def _softmax(x, axis=-1):
    return torch.softmax(x, dim=axis)
def _gelu(x):
    return torch.nn.functional.gelu(x, approximate="tanh")
def _silu(x):
    return torch.nn.functional.silu(x)
def _sigmoid(x):
    return torch.sigmoid(x)
def _layernorm(x, axis=-1, eps=1e-5):
    m = x.mean(dim=axis, keepdim=True)
    v = x.var(dim=axis, keepdim=True, unbiased=False)
    return (x - m) / torch.sqrt(v + eps)
def _scatter_add(tgt, idx, src, axis=0):
    return tgt.clone().index_add_(axis, idx, src)
def _segment_sum(src, seg, n):
    out = torch.zeros((n,) + src.shape[1:], dtype=src.dtype)
    return out.index_add_(0, seg, src)
def _pad(x, pw):
    flat = tuple(v for pair in reversed(pw) for v in pair)
    return torch.nn.functional.pad(x, flat)

kernel_name = 'concat_heads__sliding_local_dilated__mha_block__prompt'

def setup_inputs():

    _w = (lambda a: (lambda t: t.to(torch.bfloat16) if t.is_floating_point() else t)(torch.as_tensor(a)))
    def _A_setup():
        B, S, D = 1, 2048, 640
        return {
            "x": _w((lambda *s: torch.randn(*s, dtype=torch.bfloat16) * 0.4)(B, S, D)),
            "wqkv": _w((lambda *s: torch.randn(*s, dtype=torch.bfloat16) * 0.4)(D, 3 * D)),
            "wo": _w((lambda *s: torch.randn(*s, dtype=torch.bfloat16) * 0.05)(D, D)),
        }
    _A_in = _A_setup()

    def _B_setup():
        B, S, D, H = 1, 2048, 640, 32
        return {
            "x": _w((lambda *s: torch.randn(*s, dtype=torch.bfloat16) * 0.2)(B, S, D)),
            "wqkv": _w((lambda *s: torch.randn(*s, dtype=torch.bfloat16) * 0.4)(D, 3 * D)),
            "wo": _w((lambda *s: torch.randn(*s, dtype=torch.bfloat16) * 0.1)(D, D)),
        }
    _B_in = _B_setup()

    return {
        "A_x": _A_in["x"],
        "A_wqkv": _A_in["wqkv"],
        "A_wo": _A_in["wo"],
        "B_x": _B_in["x"],
        "B_wqkv": _B_in["wqkv"],
        "B_wo": _B_in["wo"],
    }

def reference(A_x, A_wqkv, A_wo, B_x, B_wqkv, B_wo):
    def _A_ref(x, wqkv, wo):
        B, S, D = x.shape
        H = 32; Dh = D // H
        W, dil = 8, 2
        qkv = (lambda x, *d: x.permute(*d))((x @ wqkv).reshape(B, S, 3, H, Dh), 2, 0, 3, 1, 4)
        q, k, v = qkv[0], qkv[1], qkv[2]
        scale = Dh ** -0.5
        pos = torch.arange(S)
        off = torch.arange(W) * dil
        idx = pos[:, None] - off[None, :]
        mask = idx >= 0
        idx_c = torch.where(mask, idx, 0).reshape(-1)
        kg = k[:, :, idx_c, :].reshape(B, H, S, W, Dh)
        vg = v[:, :, idx_c, :].reshape(B, H, S, W, Dh)
        logits = torch.einsum("bhsd,bhswd->bhsw", q, kg) * scale
        logits = torch.where(mask[None, None, :, :], logits, -1e9)
        attn = _softmax(logits, axis=-1)
        o = torch.einsum("bhsw,bhswd->bhsd", attn, vg)
        return (lambda x, *d: x.permute(*d))(o, 0, 2, 1, 3).reshape(B, S, D) @ wo

    def _B_ref(x, wqkv, wo):
        B, S, D = x.shape
        H = 32
        Dh = D // H
        qkv = (lambda x, *d: x.permute(*d))((x @ wqkv).reshape(B, S, 3, H, Dh), 2, 0, 3, 1, 4)
        q, k, v = qkv[0], qkv[1], qkv[2]
        scores = torch.einsum("bhqd,bhkd->bhqk", q, k) * (Dh ** -0.5)
        p = _softmax(scores, axis=-1)
        o = torch.einsum("bhqk,bhkd->bhqd", p, v)
        return (lambda x, *d: x.permute(*d))(o, 0, 2, 1, 3).reshape(B, S, D) @ wo

    a = _A_ref(A_x, A_wqkv, A_wo)
    b = _B_ref(B_x, B_wqkv, B_wo)
    return torch.concatenate([a, b], axis=-1)
```

```cpp
#include <hip/hip_runtime.h>
#include <hip/hip_bf16.h>

typedef __attribute__((ext_vector_type(8))) short bf16x8;
typedef __attribute__((ext_vector_type(4))) short s16x4;
typedef __attribute__((ext_vector_type(16))) float f32x16;
typedef __attribute__((ext_vector_type(2))) float f32x2;
typedef __attribute__((ext_vector_type(2))) __bf16 bf16x2;
typedef __attribute__((ext_vector_type(4))) unsigned u32x4;
typedef __attribute__((ext_vector_type(2))) unsigned u32x2;
typedef unsigned short u16;

#define DI __device__ __forceinline__
#define MFMA32(a, b, c) __builtin_amdgcn_mfma_f32_32x32x16_bf16((a), (b), (c), 0, 0, 0)
#define GPTR(p) ((const __attribute__((address_space(1))) unsigned*)(p))
#define LPTR(p) ((__attribute__((address_space(3))) unsigned*)(p))
#define WAITV(n) asm volatile("s_waitcnt vmcnt(" #n ")" ::: "memory")

constexpr int S = 2048, D = 640, N3 = 1920, H = 32, DH = 20;

constexpr size_t SZ_Q = (size_t)H * S * DH * 2;
constexpr size_t SZ_KV = (size_t)H * 64 * 3072;
constexpr size_t WS_QB = 0;
constexpr size_t WS_KVB = WS_QB + SZ_Q;
constexpr size_t WS_QA = WS_KVB + SZ_KV;
constexpr size_t WS_O = WS_QA + 3 * SZ_Q;
constexpr size_t SZ_O = (size_t)S * D * 2;

DI unsigned pk2(float lo, float hi) {
  f32x2 v = {lo, hi};
  return __builtin_bit_cast(unsigned, __builtin_convertvector(v, bf16x2));
}
DI float bf_lo(unsigned u) { return __uint_as_float(u << 16); }
DI float bf_hi(unsigned u) { return __uint_as_float(u & 0xffff0000u); }
DI float rbf(float x) { return bf_lo(pk2(x, 0.f)); }

template <int BM, int BN, int WM, int WN>
struct Gemm {
  static constexpr int TM = BM / WM / 32, TN = BN / WN / 32;
  static constexpr int A_BYTES = BM * 128, B_BYTES = 64 * BN * 2, STAGE = A_BYTES + B_BYTES;
  static constexpr int A_INS = A_BYTES / 8192, B_INS = B_BYTES / 8192;
  static constexpr int NINS = A_INS + B_INS;
  static constexpr int BROWB = BN * 2;
  static constexpr int BCH = BN / 8;
  static constexpr int LDS_BYTES = 3 * STAGE;

  static DI void stage(const u16* __restrict__ A, int lda, const u16* __restrict__ B, int ldb, int kt, char* lds, int w, int l) {
#pragma unroll
    for (int i = 0; i < A_INS; ++i) {
      const int blk = w * A_INS + i;
      const int row = blk * 8 + (l >> 3);
      const int ch = (l & 7) ^ ((row >> 1) & 7);
      __builtin_amdgcn_global_load_lds(GPTR(A + (size_t)row * lda + kt * 64 + ch * 8), LPTR(lds + blk * 1024), 16, 0, 0);
    }
#pragma unroll
    for (int i = 0; i < B_INS; ++i) {
      const int blk = w * B_INS + i;
      const int pos = blk * 64 + l;
      const int krow = pos / BCH, chp = pos % BCH;
      const int ch = chp ^ ((((krow & 3) << 2) | ((krow >> 2) & 3)) & (BCH - 1));
      __builtin_amdgcn_global_load_lds(GPTR(B + (size_t)(kt * 64 + krow) * ldb + ch * 8), LPTR(lds + A_BYTES + blk * 1024), 16, 0, 0);
    }
  }

  template <bool SWAP>
  static DI void run(const u16* __restrict__ A, int lda, const u16* __restrict__ B, int ldb, char* lds, f32x16 (&acc)[TM][TN]) {
    const int tid = threadIdx.x, w = tid >> 6, l = tid & 63;
    const int wm = w % WM, wn = w / WM;
    const int r32 = l & 31, h = l >> 5;
#pragma unroll
    for (int i = 0; i < TM; ++i)
#pragma unroll
      for (int j = 0; j < TN; ++j)
#pragma unroll
        for (int r = 0; r < 16; ++r) acc[i][j][r] = 0.f;

    int aoff[TM];
#pragma unroll
    for (int i = 0; i < TM; ++i) { const int row = wm * (TM * 32) + i * 32 + r32; aoff[i] = row * 128; }
    const int asw = ((wm * (TM * 32) + r32) >> 1) & 7;
    const int i16 = l & 15, q = i16 >> 2, p = i16 & 3, blk = (l >> 4) & 1;

    stage(A, lda, B, ldb, 0, lds, w, l);
    stage(A, lda, B, ldb, 1, lds + STAGE, w, l);
#pragma unroll 1
    for (int kt = 0; kt < 10; ++kt) {
      if (kt < 9) { if constexpr (NINS == 6) WAITV(6); else if constexpr (NINS == 4) WAITV(4); else WAITV(0); }
      else WAITV(0);
      __builtin_amdgcn_s_barrier();
      if (kt + 2 < 10) stage(A, lda, B, ldb, kt + 2, lds + ((kt + 2) % 3) * STAGE, w, l);
      const char* la = lds + (kt % 3) * STAGE;
      const char* lb = la + A_BYTES;
#pragma unroll
      for (int ks = 0; ks < 4; ++ks) {
        bf16x8 af[TM], bfr[TN];
#pragma unroll
        for (int i = 0; i < TM; ++i)
          af[i] = *reinterpret_cast<const bf16x8*>(la + aoff[i] + (((2 * ks + h) ^ asw) << 4));
#pragma unroll
        for (int j = 0; j < TN; ++j) {
          const int c0 = (wn * (TN * 32) + j * 32) / 8 + blk * 2 + (p >> 1);
          const int k0 = 16 * ks + 8 * h + q, k1 = k0 + 4;
          const int f0 = (((k0 & 3) << 2) | ((k0 >> 2) & 3)) & (BCH - 1), f1 = (((k1 & 3) << 2) | ((k1 >> 2) & 3)) & (BCH - 1);
          typedef __attribute__((address_space(3))) s16x4* lp4;
          const s16x4 lo = __builtin_amdgcn_ds_read_tr16_b64_v4i16((lp4)(lb + k0 * BROWB + ((c0 ^ f0) << 4) + 8 * (p & 1)));
          const s16x4 hi = __builtin_amdgcn_ds_read_tr16_b64_v4i16((lp4)(lb + k1 * BROWB + ((c0 ^ f1) << 4) + 8 * (p & 1)));
          bfr[j] = __builtin_shufflevector(lo, hi, 0, 1, 2, 3, 4, 5, 6, 7);
        }
#pragma unroll
        for (int i = 0; i < TM; ++i)
#pragma unroll
          for (int j = 0; j < TN; ++j)
            acc[i][j] = SWAP ? MFMA32(bfr[j], af[i], acc[i][j]) : MFMA32(af[i], bfr[j], acc[i][j]);
      }
    }
  }
};

typedef Gemm<256, 128, 4, 2> G1;

__global__ __launch_bounds__(512) void k1_qkv(const u16* __restrict__ xA, const u16* __restrict__ wA,
                                              const u16* __restrict__ xB, const u16* __restrict__ wB, char* __restrict__ ws) {
  extern __shared__ __attribute__((aligned(16))) char lds[];
  const int bid = blockIdx.x;
  const int L = (bid & 7) * 30 + (bid >> 3);
  const int br = L / 120, r = L % 120, mt = r / 15, nt = r % 15;
  const u16* A = (br ? xB : xA) + (size_t)mt * 256 * D;
  const u16* B = (br ? wB : wA) + nt * 128;
  const int sec = nt / 5;
  const int tid = threadIdx.x, w = tid >> 6, l = tid & 63, wm = w & 3, wn = w >> 2, r32 = l & 31, h = l >> 5;
  f32x16 acc[2][2];
  const bool vtype = (br == 1 && sec == 2);
  if (vtype) {
    G1::run<false>(A, D, B, N3, lds, acc);
#pragma unroll
    for (int j = 0; j < 2; ++j) {
      const int c = nt * 128 + wn * 64 + j * 32 + r32 - 1280;
      const int head = c / 20, d = c - head * 20;
#pragma unroll
      for (int i = 0; i < 2; ++i) {
        const int tile = mt * 8 + wm * 2 + i;
        char* base = ws + WS_KVB + ((size_t)head * 64 + tile) * 3072 + 1536 + h * 384 + d * 16;
#pragma unroll
        for (int s = 0; s < 2; ++s) {
          u32x4 v = {pk2(acc[i][j][8 * s], acc[i][j][8 * s + 1]), pk2(acc[i][j][8 * s + 2], acc[i][j][8 * s + 3]),
                     pk2(acc[i][j][8 * s + 4], acc[i][j][8 * s + 5]), pk2(acc[i][j][8 * s + 6], acc[i][j][8 * s + 7])};
          *reinterpret_cast<u32x4*>(base + s * 768) = v;
          if (d == 19) { u32x4 one = {0x3f803f80u, 0x3f803f80u, 0x3f803f80u, 0x3f803f80u}; *reinterpret_cast<u32x4*>(base + s * 768 + 16) = one; }
        }
      }
    }
  } else {
    G1::run<true>(A, D, B, N3, lds, acc);
    const float qs = (br == 1 && sec == 0) ? 0.22360679774997896f * 1.4426950408889634f : 1.0f;
#pragma unroll
    for (int i = 0; i < 2; ++i) {
      const int token = mt * 256 + wm * 64 + i * 32 + r32;
#pragma unroll
      for (int j = 0; j < 2; ++j) {
#pragma unroll
        for (int g = 0; g < 4; ++g) {
          const int c4 = (nt * 128 + wn * 64 + j * 32 + 8 * g + 4 * h - sec * 640) >> 2;
          const int head = c4 / 5, piece = c4 - head * 5;
          unsigned lo = pk2(acc[i][j][4 * g], acc[i][j][4 * g + 1]), hi = pk2(acc[i][j][4 * g + 2], acc[i][j][4 * g + 3]);
          if (br == 1 && sec == 1) {
            char* dst = ws + WS_KVB + ((size_t)head * 64 + (token >> 5)) * 3072 + (token & 31) * 48 + piece * 8;
            if (piece == 4) { u32x4 v = {lo, hi, 0x00003f80u, 0u}; *reinterpret_cast<u32x4*>(dst) = v; }
            else { u32x2 v = {lo, hi}; *reinterpret_cast<u32x2*>(dst) = v; }
          } else {
            if (br == 1) { lo = pk2(bf_lo(lo) * qs, bf_hi(lo) * qs); hi = pk2(bf_lo(hi) * qs, bf_hi(hi) * qs); }
            char* dst = ws + (br ? WS_QB : WS_QA + sec * SZ_Q) + ((size_t)head * S + token) * 40 + piece * 8;
            u32x2 v = {lo, hi}; *reinterpret_cast<u32x2*>(dst) = v;
          }
        }
      }
    }
  }
}

constexpr int CH_TILES = 16;
constexpr int CH_BYTES = CH_TILES * 3072;
constexpr int K2_LDS = 3 * CH_BYTES;
constexpr float MAX_SLACK = 16.f;
constexpr float L_LIMIT = 281474976710656.f;

DI void dma_chunk(const char* __restrict__ src, char* lds, int w, int l) {
#pragma unroll
  for (int i = 0; i < 6; ++i) {
    const int o = (w * 6 + i) * 1024;
    __builtin_amdgcn_global_load_lds(GPTR(src + o + l * 16), LPTR(lds + o), 16, 0, 0);
  }
}

DI bf16x8 pack8(const f32x16& x, int s) {
  u32x4 v = {pk2(x[8 * s], x[8 * s + 1]), pk2(x[8 * s + 2], x[8 * s + 3]), pk2(x[8 * s + 4], x[8 * s + 5]), pk2(x[8 * s + 6], x[8 * s + 7])};
  return __builtin_bit_cast(bf16x8, v);
}

DI void local_attn(const char* __restrict__ ws, int head, int token) {
  const u32x2* qp = reinterpret_cast<const u32x2*>(ws + WS_QA + ((size_t)head * S + token) * 40);
  const char* kb = ws + WS_QA + SZ_Q + (size_t)head * S * 40;
  const char* vb = ws + WS_QA + 2 * SZ_Q + (size_t)head * S * 40;
  float q[20];
#pragma unroll
  for (int i = 0; i < 5; ++i) { u32x2 u = qp[i]; q[4 * i] = bf_lo(u[0]); q[4 * i + 1] = bf_hi(u[0]); q[4 * i + 2] = bf_lo(u[1]); q[4 * i + 3] = bf_hi(u[1]); }
  float lg[8];
#pragma unroll
  for (int wdx = 0; wdx < 8; ++wdx) {
    const int t = token - 2 * wdx;
    const u32x2* kp = reinterpret_cast<const u32x2*>(kb + (size_t)(t < 0 ? 0 : t) * 40);
    float a = 0.f;
#pragma unroll
    for (int i = 0; i < 5; ++i) {
      u32x2 u = kp[i];
      a = fmaf(q[4 * i], bf_lo(u[0]), a); a = fmaf(q[4 * i + 1], bf_hi(u[0]), a);
      a = fmaf(q[4 * i + 2], bf_lo(u[1]), a); a = fmaf(q[4 * i + 3], bf_hi(u[1]), a);
    }
    a = rbf(rbf(a) * 0.22360679774997896f);
    lg[wdx] = (t < 0) ? rbf(-1e9f) : a;
  }
  float m = lg[0];
#pragma unroll
  for (int i = 1; i < 8; ++i) m = fmaxf(m, lg[i]);
  float sum = 0.f;
#pragma unroll
  for (int i = 0; i < 8; ++i) { lg[i] = __builtin_amdgcn_exp2f((lg[i] - m) * 1.4426950408889634f); sum += lg[i]; }
  const float inv = 1.0f / sum;
  float o[20];
#pragma unroll
  for (int i = 0; i < 20; ++i) o[i] = 0.f;
#pragma unroll
  for (int wdx = 0; wdx < 8; ++wdx) {
    const int t = token - 2 * wdx;
    const float pw = rbf(lg[wdx] * inv);
    const u32x2* vp = reinterpret_cast<const u32x2*>(vb + (size_t)(t < 0 ? 0 : t) * 40);
#pragma unroll
    for (int i = 0; i < 5; ++i) {
      u32x2 u = vp[i];
      o[4 * i] = fmaf(pw, bf_lo(u[0]), o[4 * i]); o[4 * i + 1] = fmaf(pw, bf_hi(u[0]), o[4 * i + 1]);
      o[4 * i + 2] = fmaf(pw, bf_lo(u[1]), o[4 * i + 2]); o[4 * i + 3] = fmaf(pw, bf_hi(u[1]), o[4 * i + 3]);
    }
  }
  u32x2* op = reinterpret_cast<u32x2*>((char*)ws + WS_O + ((size_t)token * D + head * 20) * 2);
#pragma unroll
  for (int i = 0; i < 5; ++i) { u32x2 u = {pk2(o[4 * i], o[4 * i + 1]), pk2(o[4 * i + 2], o[4 * i + 3])}; op[i] = u; }
}

__global__ __launch_bounds__(512) void k2_attn(char* __restrict__ ws) {
  extern __shared__ __attribute__((aligned(16))) char lds[];
  const int bid = blockIdx.x;
  const int head = (bid & 7) * 4 + ((bid >> 3) & 3), qt = bid >> 5;
  const int tid = threadIdx.x, w = tid >> 6, l = tid & 63, r32 = l & 31, h = l >> 5;
  const char* kv = ws + WS_KVB + (size_t)head * 64 * 3072;

  dma_chunk(kv, lds, w, l);
  dma_chunk(kv + CH_BYTES, lds + CH_BYTES, w, l);
  dma_chunk(kv + 2 * CH_BYTES, lds + 2 * CH_BYTES, w, l);

  const int token = qt * 256 + w * 32 + r32;
  u32x4 q0, q1;
  {
    const u32x2* qp = reinterpret_cast<const u32x2*>(ws + WS_QB + ((size_t)head * S + token) * 40);
    const u32x2 a = qp[2 * h], b = qp[2 * h + 1], c = qp[4];
    q0 = u32x4{a[0], a[1], b[0], b[1]};
    q1 = h ? u32x4{0u, 0u, 0u, 0u} : u32x4{c[0], c[1], 0u, 0u};
  }
  if (tid < 256) local_attn(ws, head, qt * 256 + tid);

  const int koff = r32 * 48, voff = 1536 + h * 384 + (r32 < 20 ? r32 : 20) * 16;
  f32x16 o;
#pragma unroll
  for (int r = 0; r < 16; ++r) o[r] = 0.f;
  const f32x16 zero = o;

#pragma unroll 1
  for (int c = 0; c < 4; ++c) {
    if (c == 0) WAITV(12); else if (c == 1) WAITV(12); else if (c == 2) WAITV(6); else WAITV(0);
    __builtin_amdgcn_s_barrier();
    if (c == 1) dma_chunk(kv + 3 * CH_BYTES, lds, w, l);
    const char* lb = lds + (c % 3) * CH_BYTES;
    if (c == 0) {
      const bf16x8 kf0 = *reinterpret_cast<const bf16x8*>(lb + koff + 16 * h);
      const bf16x8 kf1 = *reinterpret_cast<const bf16x8*>(lb + koff + 32);
      f32x16 s = MFMA32(kf0, __builtin_bit_cast(bf16x8, q0), zero);
      s = MFMA32(kf1, __builtin_bit_cast(bf16x8, q1), s);
      float m = s[0];
#pragma unroll
      for (int r = 1; r < 16; ++r) m = fmaxf(m, s[r]);
      m = fmaxf(m, __shfl_xor(m, 32));
      if (h == 0) q1[2] = pk2(-(m + MAX_SLACK), 0.f);
    }
#pragma unroll 4
    for (int t = 0; t < CH_TILES; ++t) {
      const char* tb = lb + t * 3072;
      const bf16x8 kf0 = *reinterpret_cast<const bf16x8*>(tb + koff + 16 * h);
      const bf16x8 kf1 = *reinterpret_cast<const bf16x8*>(tb + koff + 32);
      const bf16x8 vf0 = *reinterpret_cast<const bf16x8*>(tb + voff);
      const bf16x8 vf1 = *reinterpret_cast<const bf16x8*>(tb + voff + 768);
      f32x16 s = MFMA32(kf0, __builtin_bit_cast(bf16x8, q0), zero);
      s = MFMA32(kf1, __builtin_bit_cast(bf16x8, q1), s);
#pragma unroll
      for (int r = 0; r < 16; ++r) s[r] = __builtin_amdgcn_exp2f(s[r]);
      o = MFMA32(vf0, pack8(s, 0), o);
      o = MFMA32(vf1, pack8(s, 1), o);
    }
  }
  float lsum = __shfl_xor(o[8], 32);
  if (h) lsum = o[8];
  if (__any(!(lsum < L_LIMIT))) {
    if (h == 0) q1[2] = 0u;
    float m = -3.0e38f;
#pragma unroll
    for (int r = 0; r < 16; ++r) o[r] = 0.f;
#pragma unroll 1
    for (int t = 0; t < 64; ++t) {
      const char* tb = kv + t * 3072;
      const bf16x8 kf0 = *reinterpret_cast<const bf16x8*>(tb + koff + 16 * h);
      const bf16x8 kf1 = *reinterpret_cast<const bf16x8*>(tb + koff + 32);
      const bf16x8 vf0 = *reinterpret_cast<const bf16x8*>(tb + voff);
      const bf16x8 vf1 = *reinterpret_cast<const bf16x8*>(tb + voff + 768);
      f32x16 s = MFMA32(kf0, __builtin_bit_cast(bf16x8, q0), zero);
      s = MFMA32(kf1, __builtin_bit_cast(bf16x8, q1), s);
      float tm = s[0];
#pragma unroll
      for (int r = 1; r < 16; ++r) tm = fmaxf(tm, s[r]);
      tm = fmaxf(tm, __shfl_xor(tm, 32));
      const float mn = fmaxf(m, tm);
      const float alpha = __builtin_amdgcn_exp2f(m - mn);
      m = mn;
#pragma unroll
      for (int r = 0; r < 16; ++r) { o[r] *= alpha; s[r] = __builtin_amdgcn_exp2f(s[r] - mn); }
      o = MFMA32(vf0, pack8(s, 0), o);
      o = MFMA32(vf1, pack8(s, 1), o);
    }
    lsum = __shfl_xor(o[8], 32);
    if (h) lsum = o[8];
  }
  const float inv = 1.0f / lsum;
  char* op = ws + WS_O + SZ_O + ((size_t)token * D + head * 20 + 4 * h) * 2;
  { u32x2 u = {pk2(o[0] * inv, o[1] * inv), pk2(o[2] * inv, o[3] * inv)}; *reinterpret_cast<u32x2*>(op) = u; }
  { u32x2 u = {pk2(o[4] * inv, o[5] * inv), pk2(o[6] * inv, o[7] * inv)}; *reinterpret_cast<u32x2*>(op + 16) = u; }
  if (h == 0) { u32x2 u = {pk2(o[8] * inv, o[9] * inv), pk2(o[10] * inv, o[11] * inv)}; *reinterpret_cast<u32x2*>(op + 32) = u; }
}

typedef Gemm<128, 128, 4, 2> G3;

__global__ __launch_bounds__(512) void k3_out(const char* __restrict__ ws, const u16* __restrict__ woA, const u16* __restrict__ woB,
                                              u16* __restrict__ out) {
  extern __shared__ __attribute__((aligned(16))) char lds[];
  const int bid = blockIdx.x;
  const int L = (bid & 7) * 20 + (bid >> 3);
  const int br = L / 80, r = L % 80, mt = r / 5, nt = r % 5;
  const u16* A = reinterpret_cast<const u16*>(ws + WS_O + br * SZ_O) + (size_t)mt * 128 * D;
  const u16* B = (br ? woB : woA) + nt * 128;
  const int tid = threadIdx.x, w = tid >> 6, l = tid & 63, wm = w & 3, wn = w >> 2, r32 = l & 31, h = l >> 5;
  f32x16 acc[1][2];
  G3::run<true>(A, D, B, D, lds, acc);
  const int token = mt * 128 + wm * 32 + r32;
#pragma unroll
  for (int j = 0; j < 2; ++j)
#pragma unroll
    for (int g = 0; g < 4; ++g) {
      const int n = nt * 128 + wn * 64 + j * 32 + 8 * g + 4 * h;
      u32x2 v = {pk2(acc[0][j][4 * g], acc[0][j][4 * g + 1]), pk2(acc[0][j][4 * g + 2], acc[0][j][4 * g + 3])};
      *reinterpret_cast<u32x2*>(out + (size_t)token * 1280 + br * 640 + n) = v;
    }
}

extern "C" void kernel_launch(void* const* d_in, const int* in_sizes, int n_in,
                              void* d_out, int out_size, void* d_ws, size_t ws_size,
                              hipStream_t stream) {
  const u16* xA = (const u16*)d_in[0];
  const u16* wA = (const u16*)d_in[1];
  const u16* woA = (const u16*)d_in[2];
  const u16* xB = (const u16*)d_in[3];
  const u16* wB = (const u16*)d_in[4];
  const u16* woB = (const u16*)d_in[5];
  char* ws = (char*)d_ws;
  hipFuncSetAttribute((const void*)k1_qkv, hipFuncAttributeMaxDynamicSharedMemorySize, G1::LDS_BYTES);
  hipFuncSetAttribute((const void*)k2_attn, hipFuncAttributeMaxDynamicSharedMemorySize, K2_LDS);
  hipFuncSetAttribute((const void*)k3_out, hipFuncAttributeMaxDynamicSharedMemorySize, G3::LDS_BYTES);
  k1_qkv<<<240, 512, G1::LDS_BYTES, stream>>>(xA, wA, xB, wB, ws);
  k2_attn<<<256, 512, K2_LDS, stream>>>(ws);
  k3_out<<<160, 512, G3::LDS_BYTES, stream>>>(ws, woA, woB, (u16*)d_out);
}
```

```cpp
#include <hip/hip_runtime.h>
#include <hip/hip_bf16.h>

typedef __attribute__((ext_vector_type(8))) short bf16x8;
typedef __attribute__((ext_vector_type(4))) short s16x4;
typedef __attribute__((ext_vector_type(16))) float f32x16;
typedef __attribute__((ext_vector_type(2))) float f32x2;
typedef __attribute__((ext_vector_type(2))) __bf16 bf16x2;
typedef __attribute__((ext_vector_type(4))) unsigned u32x4;
typedef __attribute__((ext_vector_type(2))) unsigned u32x2;
typedef unsigned short u16;

#define DI __device__ __forceinline__
#define MFMA32(a, b, c) __builtin_amdgcn_mfma_f32_32x32x16_bf16((a), (b), (c), 0, 0, 0)
#define GPTR(p) ((const __attribute__((address_space(1))) unsigned*)(p))
#define LPTR(p) ((__attribute__((address_space(3))) unsigned*)(p))
#define WAITV(n) asm volatile("s_waitcnt vmcnt(%0)" ::"n"(n) : "memory")
#define WAITL0() asm volatile("s_waitcnt lgkmcnt(0)" ::: "memory")
#define BAR() do { asm volatile("" ::: "memory"); __builtin_amdgcn_s_barrier(); asm volatile("" ::: "memory"); } while (0)
#define DMA16(src, dst) __builtin_amdgcn_global_load_lds(GPTR(src), LPTR(dst), 16, 0, 0)

constexpr int S = 2048, D = 640, N3 = 1920, H = 32;

constexpr size_t SZ_P = (size_t)160 * S * 8;
constexpr int KV_TILE = 2816;
constexpr size_t SZ_KV = (size_t)H * 64 * KV_TILE;
constexpr size_t WS_QB = 0;
constexpr size_t WS_KVB = WS_QB + SZ_P;
constexpr size_t WS_QA = WS_KVB + SZ_KV;
constexpr size_t WS_O = WS_QA + 3 * SZ_P;

DI unsigned pk2(float lo, float hi) {
  f32x2 v = {lo, hi};
  return __builtin_bit_cast(unsigned, __builtin_convertvector(v, bf16x2));
}
DI float bf_lo(unsigned u) { return __uint_as_float(u << 16); }
DI float bf_hi(unsigned u) { return __uint_as_float(u & 0xffff0000u); }
DI float rbf(float x) { return bf_lo(pk2(x, 0.f)); }

template <int BM, int BN, int WM, int WN, int AMODE>
struct Gemm {
  static constexpr int TM = BM / WM / 32, TN = BN / WN / 32;
  static constexpr int A_BYTES = BM * 128, B_BYTES = 64 * BN * 2, STAGE = A_BYTES + B_BYTES;
  static constexpr int A_INS = A_BYTES / 8192, B_INS = B_BYTES / 8192;
  static constexpr int NINS = A_INS + B_INS;
  static constexpr int BROWB = BN * 2;
  static constexpr int BCH = BN / 8;
  static constexpr int LDS_BYTES = 3 * STAGE;
  struct Frag { bf16x8 a[TM]; bf16x8 b[TN]; };

  static DI void stage(const u16* __restrict__ A, const u16* __restrict__ B, int ldb, int kt, char* lds, int w, int l) {
#pragma unroll
    for (int i = 0; i < A_INS; ++i) {
      const int blk = w * A_INS + i;
      if constexpr (AMODE == 0) {
        const int row = blk * 8 + (l >> 3);
        const int ch = (l & 7) ^ ((row >> 1) & 7);
        DMA16(A + (size_t)row * D + kt * 64 + ch * 8, lds + blk * 1024);
      } else {
        const int piece = blk / (BM / 128), part = blk % (BM / 128);
        DMA16(A + ((size_t)(kt * 16 + piece) * S + part * 128) * 4 + l * 8, lds + blk * 1024);
      }
    }
#pragma unroll
    for (int i = 0; i < B_INS; ++i) {
      const int blk = w * B_INS + i;
      const int pos = blk * 64 + l;
      const int krow = pos / BCH, chp = pos % BCH;
      const int ch = chp ^ (((krow & 3) << 2) | ((krow >> 2) & 3));
      DMA16(B + (size_t)(kt * 64 + krow) * ldb + ch * 8, lds + A_BYTES + blk * 1024);
    }
  }

  static DI void load_frag(Frag& f, const char* la, int ks, int wm, int wn, int l) {
    const int r32 = l & 31, h = l >> 5;
    typedef __attribute__((address_space(3))) s16x4* lp4;
#pragma unroll
    for (int i = 0; i < TM; ++i) {
      const int row = wm * (TM * 32) + i * 32 + r32;
      if constexpr (AMODE == 0) {
        f.a[i] = *reinterpret_cast<const bf16x8*>(la + row * 128 + (((2 * ks + h) ^ ((row >> 1) & 7)) << 4));
      } else {
        const s16x4 lo = *reinterpret_cast<const s16x4*>(la + ((4 * ks + 2 * h) * BM + row) * 8);
        const s16x4 hi = *reinterpret_cast<const s16x4*>(la + ((4 * ks + 2 * h + 1) * BM + row) * 8);
        f.a[i] = __builtin_shufflevector(lo, hi, 0, 1, 2, 3, 4, 5, 6, 7);
      }
    }
    const char* lb = la + A_BYTES;
    const int i16 = l & 15, q = i16 >> 2, p = i16 & 3, blk = (l >> 4) & 1;
#pragma unroll
    for (int j = 0; j < TN; ++j) {
      const int c0 = (wn * (TN * 32) + j * 32) / 8 + blk * 2 + (p >> 1);
      const int k0 = 16 * ks + 8 * h + q, k1 = k0 + 4;
      const int f0 = ((k0 & 3) << 2) | ((k0 >> 2) & 3), f1 = ((k1 & 3) << 2) | ((k1 >> 2) & 3);
      const s16x4 lo = __builtin_amdgcn_ds_read_tr16_b64_v4i16((lp4)(lb + k0 * BROWB + ((c0 ^ f0) << 4) + 8 * (p & 1)));
      const s16x4 hi = __builtin_amdgcn_ds_read_tr16_b64_v4i16((lp4)(lb + k1 * BROWB + ((c0 ^ f1) << 4) + 8 * (p & 1)));
      f.b[j] = __builtin_shufflevector(lo, hi, 0, 1, 2, 3, 4, 5, 6, 7);
    }
  }

  template <bool SWAP>
  static DI void mma(const Frag& f, f32x16 (&acc)[TM][TN]) {
#pragma unroll
    for (int i = 0; i < TM; ++i)
#pragma unroll
      for (int j = 0; j < TN; ++j)
        acc[i][j] = SWAP ? MFMA32(f.b[j], f.a[i], acc[i][j]) : MFMA32(f.a[i], f.b[j], acc[i][j]);
  }

  template <bool SWAP>
  static DI void run(const u16* __restrict__ A, const u16* __restrict__ B, int ldb, int rot, char* lds, f32x16 (&acc)[TM][TN]) {
    const int tid = threadIdx.x, w = tid >> 6, l = tid & 63;
    const int wm = w % WM, wn = w / WM;
#pragma unroll
    for (int i = 0; i < TM; ++i)
#pragma unroll
      for (int j = 0; j < TN; ++j)
#pragma unroll
        for (int r = 0; r < 16; ++r) acc[i][j][r] = 0.f;
    Frag fa, fb;
#pragma unroll
    for (int i = 0; i < TM; ++i) fa.a[i] = bf16x8{0, 0, 0, 0, 0, 0, 0, 0};
#pragma unroll
    for (int j = 0; j < TN; ++j) fa.b[j] = bf16x8{0, 0, 0, 0, 0, 0, 0, 0};

    int k2 = rot;
    stage(A, B, ldb, k2, lds, w, l); k2 = (k2 == 9) ? 0 : k2 + 1;
    stage(A, B, ldb, k2, lds + STAGE, w, l); k2 = (k2 == 9) ? 0 : k2 + 1;
    int cur = 0, nxt = 2 * STAGE;
#pragma unroll 1
    for (int it = 0; it < 10; ++it) {
      WAITL0();
      if (it < 9) WAITV(NINS); else WAITV(0);
      BAR();
      const char* la = lds + cur;
      load_frag(fb, la, 0, wm, wn, l);
      if (it < 8) { stage(A, B, ldb, k2, lds + nxt, w, l); k2 = (k2 == 9) ? 0 : k2 + 1; }
      mma<SWAP>(fa, acc);
      load_frag(fa, la, 1, wm, wn, l);
      mma<SWAP>(fb, acc);
      load_frag(fb, la, 2, wm, wn, l);
      mma<SWAP>(fa, acc);
      load_frag(fa, la, 3, wm, wn, l);
      mma<SWAP>(fb, acc);
      nxt = cur; cur = (cur == 2 * STAGE) ? 0 : cur + STAGE;
    }
    mma<SWAP>(fa, acc);
  }
};

typedef Gemm<256, 128, 4, 2, 0> G1;

__global__ __launch_bounds__(512) void k1_qkv(const u16* __restrict__ xA, const u16* __restrict__ wA,
                                              const u16* __restrict__ xB, const u16* __restrict__ wB, char* __restrict__ ws) {
  extern __shared__ __attribute__((aligned(16))) char lds[];
  const int bid = blockIdx.x;
  const int L = (bid & 7) * 30 + (bid >> 3);
  const int br = L / 120, r = L % 120, mt = r / 15, nt = r % 15;
  const u16* A = (br ? xB : xA) + (size_t)mt * 256 * D;
  const u16* B = (br ? wB : wA) + nt * 128;
  const int sec = nt / 5;
  const int rot = (nt + 5 * (mt & 1)) % 10;
  const int tid = threadIdx.x, w = tid >> 6, l = tid & 63, wm = w & 3, wn = w >> 2, r32 = l & 31, h = l >> 5;
  f32x16 acc[2][2];
  const bool vtype = (br == 1 && sec == 2);
  if (vtype) {
    G1::run<false>(A, B, N3, rot, lds, acc);
#pragma unroll
    for (int j = 0; j < 2; ++j) {
      const int c = nt * 128 + wn * 64 + j * 32 + r32 - 1280;
      const int head = c / 20, d = c - head * 20;
#pragma unroll
      for (int i = 0; i < 2; ++i) {
        const int tile = mt * 8 + wm * 2 + i;
        char* base = ws + WS_KVB + ((size_t)head * 64 + tile) * KV_TILE + 1280 + h * 384 + d * 16;
#pragma unroll
        for (int s = 0; s < 2; ++s) {
          u32x4 v = {pk2(acc[i][j][8 * s], acc[i][j][8 * s + 1]), pk2(acc[i][j][8 * s + 2], acc[i][j][8 * s + 3]),
                     pk2(acc[i][j][8 * s + 4], acc[i][j][8 * s + 5]), pk2(acc[i][j][8 * s + 6], acc[i][j][8 * s + 7])};
          *reinterpret_cast<u32x4*>(base + s * 768) = v;
          if (d == 19) { u32x4 one = {0x3f803f80u, 0x3f803f80u, 0x3f803f80u, 0x3f803f80u}; *reinterpret_cast<u32x4*>(base + s * 768 + 16) = one; }
        }
      }
    }
  } else {
    G1::run<true>(A, B, N3, rot, lds, acc);
    const bool isKB = (br == 1 && sec == 1), isQB = (br == 1 && sec == 0);
    const float qs = 0.22360679774997896f * 1.4426950408889634f;
    char* pbase = ws + (br ? WS_QB : WS_QA + sec * SZ_P);
#pragma unroll
    for (int i = 0; i < 2; ++i) {
      const int token = mt * 256 + wm * 64 + i * 32 + r32;
#pragma unroll
      for (int j = 0; j < 2; ++j) {
#pragma unroll
        for (int g = 0; g < 4; ++g) {
          const int c4 = (nt * 128 + wn * 64 + j * 32 + 8 * g + 4 * h - sec * 640) >> 2;
          unsigned lo = pk2(acc[i][j][4 * g], acc[i][j][4 * g + 1]), hi = pk2(acc[i][j][4 * g + 2], acc[i][j][4 * g + 3]);
          char* dst;
          if (isKB) {
            const int head = c4 / 5, piece = c4 - head * 5;
            dst = ws + WS_KVB + ((size_t)head * 64 + (token >> 5)) * KV_TILE + piece * 256 + r32 * 8;
          } else {
            if (isQB) { lo = pk2(bf_lo(lo) * qs, bf_hi(lo) * qs); hi = pk2(bf_lo(hi) * qs, bf_hi(hi) * qs); }
            dst = pbase + ((size_t)c4 * S + token) * 8;
          }
          u32x2 v = {lo, hi}; *reinterpret_cast<u32x2*>(dst) = v;
        }
      }
    }
  }
}

#define K2_NW 16
constexpr int NW = K2_NW;
constexpr int KG = NW / 8;
constexpr int CH_KB = 44;
constexpr int CH_BYTES = CH_KB * 1024;
constexpr int CH_INS = 48 / NW;
constexpr int LOC_INS = 64 / NW;
constexpr int K2_LDS = 2 * CH_BYTES + 50 * 1024;
constexpr int LQ_OFF = 2 * CH_BYTES, LK_OFF = LQ_OFF + 10240, LV_OFF = LK_OFF + 15360, QB_OFF = LV_OFF + 15360;
constexpr float MAX_SLACK = 16.f;
constexpr float L_LIMIT = 281474976710656.f;

DI void dma_chunk(const char* __restrict__ kv, int kb0, char* lds, int w, int l) {
#pragma unroll
  for (int i = 0; i < CH_INS; ++i) {
    int o = w * CH_INS + i;
    o = o < CH_KB ? o : CH_KB - 1;
    int kb = kb0 + o;
    kb = kb >= 176 ? kb - 176 : kb;
    DMA16(kv + kb * 1024 + l * 16, lds + o * 1024);
  }
}

DI bf16x8 pack8(const f32x16& x, int s) {
  u32x4 v = {pk2(x[8 * s], x[8 * s + 1]), pk2(x[8 * s + 2], x[8 * s + 3]), pk2(x[8 * s + 4], x[8 * s + 5]), pk2(x[8 * s + 6], x[8 * s + 7])};
  return __builtin_bit_cast(bf16x8, v);
}

struct KF { u32x4 k0, k1; };
struct VF { bf16x8 v0, v1; };
DI KF load_kf(const char* tb, int koff) {
  const u32x2 a = *reinterpret_cast<const u32x2*>(tb + koff);
  const u32x2 b = *reinterpret_cast<const u32x2*>(tb + koff + 256);
  const u32x2 c = *reinterpret_cast<const u32x2*>(tb + (koff & 255) + 1024);
  KF f; f.k0 = u32x4{a[0], a[1], b[0], b[1]}; f.k1 = u32x4{c[0], c[1], 0x00003f80u, 0u};
  return f;
}
DI VF load_vf(const char* tb, int voff) {
  VF f; f.v0 = *reinterpret_cast<const bf16x8*>(tb + voff); f.v1 = *reinterpret_cast<const bf16x8*>(tb + voff + 768);
  return f;
}
DI f32x16 qk(const KF& k, const u32x4& q0, const u32x4& q1) {
  f32x16 z;
#pragma unroll
  for (int r = 0; r < 16; ++r) z[r] = 0.f;
  f32x16 s = MFMA32(__builtin_bit_cast(bf16x8, k.k0), __builtin_bit_cast(bf16x8, q0), z);
  return MFMA32(__builtin_bit_cast(bf16x8, k.k1), __builtin_bit_cast(bf16x8, q1), s);
}

__global__ __launch_bounds__(64 * NW) void k2_attn(char* __restrict__ ws) {
  extern __shared__ __attribute__((aligned(16))) char lds[];
  const int bid = blockIdx.x;
  const int head = (bid & 7) * 4 + ((bid >> 3) & 3), qt = bid >> 5;
  const int tid = threadIdx.x, w = tid >> 6, l = tid & 63, r32 = l & 31, h = l >> 5;
  const int qs = w & 7, kg = w >> 3;
  const char* kv = ws + WS_KVB + (size_t)head * 64 * KV_TILE;
  const int t0 = qt * 256;
  const int kb_start = qt * 22;

#pragma unroll
  for (int i = 0; i < LOC_INS; ++i) {
    int o = w * LOC_INS + i;
    o = o < 50 ? o : 49;
    const char* src;
    if (o < 10) src = ws + WS_QA + ((size_t)(head * 5 + (o >> 1)) * S + t0) * 8 + (o & 1) * 1024;
    else if (o < 25) { const int e = o - 10, pc = e / 3; src = ws + WS_QA + SZ_P + ((size_t)(head * 5 + pc) * S + t0 - 16) * 8 + (e - pc * 3) * 1024; }
    else if (o < 40) { const int e = o - 25, pc = e / 3; src = ws + WS_QA + 2 * SZ_P + ((size_t)(head * 5 + pc) * S + t0 - 16) * 8 + (e - pc * 3) * 1024; }
    else { const int e = o - 40; src = ws + WS_QB + ((size_t)(head * 5 + (e >> 1)) * S + t0) * 8 + (e & 1) * 1024; }
    DMA16(src + l * 16, lds + LQ_OFF + o * 1024);
  }
  dma_chunk(kv, kb_start, lds, w, l);
  WAITV(CH_INS);
  BAR();

  u32x4 q0, q1;
  {
    const char* qp = lds + QB_OFF + (qs * 32 + r32) * 8;
    const u32x2 a = *reinterpret_cast<const u32x2*>(qp + (2 * h) * 2048), b = *reinterpret_cast<const u32x2*>(qp + (2 * h + 1) * 2048);
    const u32x2 c = *reinterpret_cast<const u32x2*>(qp + 4 * 2048);
    q0 = u32x4{a[0], a[1], b[0], b[1]};
    q1 = h ? u32x4{0u, 0u, 0u, 0u} : u32x4{c[0], c[1], 0u, 0u};
  }

  {
    constexpr int TPT = NW / 4;
    constexpr int WPT = 8 / TPT;
    const int tl = tid / TPT, sub = tid % TPT;
    const int token = t0 + tl;
    float q[20];
#pragma unroll
    for (int i = 0; i < 5; ++i) {
      const u32x2 u = *reinterpret_cast<const u32x2*>(lds + LQ_OFF + i * 2048 + tl * 8);
      q[4 * i] = bf_lo(u[0]); q[4 * i + 1] = bf_hi(u[0]); q[4 * i + 2] = bf_lo(u[1]); q[4 * i + 3] = bf_hi(u[1]);
    }
    float lg[WPT];
#pragma unroll
    for (int j = 0; j < WPT; ++j) {
      const int wdx = sub * WPT + j;
      const int row = tl + 16 - 2 * wdx;
      float a = 0.f;
#pragma unroll
      for (int i = 0; i < 5; ++i) {
        const u32x2 u = *reinterpret_cast<const u32x2*>(lds + LK_OFF + i * 3072 + row * 8);
        a = fmaf(q[4 * i], bf_lo(u[0]), a); a = fmaf(q[4 * i + 1], bf_hi(u[0]), a);
        a = fmaf(q[4 * i + 2], bf_lo(u[1]), a); a = fmaf(q[4 * i + 3], bf_hi(u[1]), a);
      }
      a = rbf(rbf(a) * 0.22360679774997896f);
      lg[j] = (token - 2 * wdx < 0) ? rbf(-1e9f) : a;
    }
    float m = lg[0];
#pragma unroll
    for (int j = 1; j < WPT; ++j) m = fmaxf(m, lg[j]);
#pragma unroll
    for (int x = 1; x < TPT; x <<= 1) m = fmaxf(m, __shfl_xor(m, x));
    float sum = 0.f;
#pragma unroll
    for (int j = 0; j < WPT; ++j) { lg[j] = __builtin_amdgcn_exp2f((lg[j] - m) * 1.4426950408889634f); sum += lg[j]; }
#pragma unroll
    for (int x = 1; x < TPT; x <<= 1) sum += __shfl_xor(sum, x);
    const float inv = 1.0f / sum;
    float o[20];
#pragma unroll
    for (int i = 0; i < 20; ++i) o[i] = 0.f;
#pragma unroll
    for (int j = 0; j < WPT; ++j) {
      const int wdx = sub * WPT + j;
      const bool ok = (token - 2 * wdx >= 0);
      const int row = ok ? tl + 16 - 2 * wdx : 16;
      const float pw = rbf(lg[j] * inv);
#pragma unroll
      for (int i = 0; i < 5; ++i) {
        const u32x2 u = *reinterpret_cast<const u32x2*>(lds + LV_OFF + i * 3072 + row * 8);
        o[4 * i] = fmaf(pw, bf_lo(u[0]), o[4 * i]); o[4 * i + 1] = fmaf(pw, bf_hi(u[0]), o[4 * i + 1]);
        o[4 * i + 2] = fmaf(pw, bf_lo(u[1]), o[4 * i + 2]); o[4 * i + 3] = fmaf(pw, bf_hi(u[1]), o[4 * i + 3]);
      }
    }
#pragma unroll
    for (int x = 1; x < TPT; x <<= 1)
#pragma unroll
      for (int i = 0; i < 20; ++i) o[i] += __shfl_xor(o[i], x);
    char* ob = ws + WS_O + ((size_t)head * 5 * S + token) * 8;
#pragma unroll
    for (int i = 0; i < 5; ++i) {
      if ((i % TPT) == sub) { u32x2 u = {pk2(o[4 * i], o[4 * i + 1]), pk2(o[4 * i + 2], o[4 * i + 3])}; *reinterpret_cast<u32x2*>(ob + (size_t)i * S * 8) = u; }
    }
  }

  dma_chunk(kv, kb_start + CH_KB, lds + CH_BYTES, w, l);
  WAITL0();
  WAITV(CH_INS);
  BAR();
  dma_chunk(kv, kb_start + 2 * CH_KB, lds + 2 * CH_BYTES, w, l);

  const int koff = (2 * h) * 256 + r32 * 8, voff = 1280 + h * 384 + (r32 < 20 ? r32 : 20) * 16;
  {
    const f32x16 s = qk(load_kf(lds, koff), q0, q1);
    float m = s[0];
#pragma unroll
    for (int r = 1; r < 16; ++r) m = fmaxf(m, s[r]);
    m = fmaxf(m, __shfl_xor(m, 32));
    if (h == 0) q1[2] = pk2(-(m + MAX_SLACK), 0.f);
  }
  f32x16 o;
#pragma unroll
  for (int r = 0; r < 16; ++r) o[r] = 0.f;

  constexpr int NT = 16 / KG;
#pragma unroll 1
  for (int c = 0; c < 4; ++c) {
    if (c > 0) {
      if (c < 3) WAITV(CH_INS); else WAITV(0);
      BAR();
      if (c == 1) dma_chunk(kv, kb_start + 3 * CH_KB, lds, w, l);
      if (c == 2) {
      }
    }
    const char* lb = lds + (c == 3 ? 0 : c) * CH_BYTES + kg * KV_TILE;
    f32x16 sc = qk(load_kf(lb, koff), q0, q1);
#pragma unroll
    for (int t = 0; t < NT; ++t) {
      const char* tb = lb + t * (KG * KV_TILE);
      const VF vf = load_vf(tb, voff);
      f32x16 sn;
      if (t + 1 < NT) sn = qk(load_kf(tb + KG * KV_TILE, koff), q0, q1);
#pragma unroll
      for (int r = 0; r < 16; ++r) sc[r] = __builtin_amdgcn_exp2f(sc[r]);
      o = MFMA32(vf.v0, pack8(sc, 0), o);
      o = MFMA32(vf.v1, pack8(sc, 1), o);
      if (t + 1 < NT) sc = sn;
    }
    WAITL0();
  }

  if constexpr (KG == 2) {
    BAR();
    float* xb = reinterpret_cast<float*>(lds) + qs * (12 * 64) + l;
    if (kg == 1) {
#pragma unroll
      for (int r = 0; r < 12; ++r) xb[r * 64] = o[r];
    }
    __syncthreads();
    if (kg == 1) return;
#pragma unroll
    for (int r = 0; r < 12; ++r) o[r] += xb[r * 64];
  }

  float lsum = __shfl_xor(o[8], 32);
  if (h) lsum = o[8];
  if (__any(!(lsum < L_LIMIT))) {
    if (h == 0) q1[2] = 0u;
    float m = -3.0e38f;
#pragma unroll
    for (int r = 0; r < 16; ++r) o[r] = 0.f;
#pragma unroll 1
    for (int t = 0; t < 64; ++t) {
      const char* tb = kv + t * KV_TILE;
      f32x16 s = qk(load_kf(tb, koff), q0, q1);
      const VF vf = load_vf(tb, voff);
      float tm = s[0];
#pragma unroll
      for (int r = 1; r < 16; ++r) tm = fmaxf(tm, s[r]);
      tm = fmaxf(tm, __shfl_xor(tm, 32));
      const float mn = fmaxf(m, tm);
      const float alpha = __builtin_amdgcn_exp2f(m - mn);
      m = mn;
#pragma unroll
      for (int r = 0; r < 16; ++r) { o[r] *= alpha; s[r] = __builtin_amdgcn_exp2f(s[r] - mn); }
      o = MFMA32(vf.v0, pack8(s, 0), o);
      o = MFMA32(vf.v1, pack8(s, 1), o);
    }
    lsum = __shfl_xor(o[8], 32);
    if (h) lsum = o[8];
  }
  const float inv = 1.0f / lsum;
  const int token = t0 + qs * 32 + r32;
  char* op = ws + WS_O + SZ_P + ((size_t)(head * 5 + h) * S + token) * 8;
  { u32x2 u = {pk2(o[0] * inv, o[1] * inv), pk2(o[2] * inv, o[3] * inv)}; *reinterpret_cast<u32x2*>(op) = u; }
  { u32x2 u = {pk2(o[4] * inv, o[5] * inv), pk2(o[6] * inv, o[7] * inv)}; *reinterpret_cast<u32x2*>(op + (size_t)2 * S * 8) = u; }
  if (h == 0) { u32x2 u = {pk2(o[8] * inv, o[9] * inv), pk2(o[10] * inv, o[11] * inv)}; *reinterpret_cast<u32x2*>(op + (size_t)4 * S * 8) = u; }
}

typedef Gemm<128, 128, 4, 2, 1> G3;

__global__ __launch_bounds__(512) void k3_out(const char* __restrict__ ws, const u16* __restrict__ woA, const u16* __restrict__ woB,
                                              u16* __restrict__ out) {
  extern __shared__ __attribute__((aligned(16))) char lds[];
  const int bid = blockIdx.x;
  const int L = (bid & 7) * 20 + (bid >> 3);
  const int br = L / 80, r = L % 80, mt = r / 5, nt = r % 5;
  const u16* A = reinterpret_cast<const u16*>(ws + WS_O + br * SZ_P) + (size_t)mt * 128 * 4;
  const u16* B = (br ? woB : woA) + nt * 128;
  const int rot = (2 * nt + 5 * (mt & 1) + ((mt >> 1) & 1)) % 10;
  const int tid = threadIdx.x, w = tid >> 6, l = tid & 63, wm = w & 3, wn = w >> 2, r32 = l & 31, h = l >> 5;
  f32x16 acc[1][2];
  G3::run<true>(A, B, D, rot, lds, acc);
  WAITL0();
  BAR();
  char* wl = lds + w * (32 * 144);
#pragma unroll
  for (int j = 0; j < 2; ++j)
#pragma unroll
    for (int g = 0; g < 4; ++g) {
      u32x2 v = {pk2(acc[0][j][4 * g], acc[0][j][4 * g + 1]), pk2(acc[0][j][4 * g + 2], acc[0][j][4 * g + 3])};
      *reinterpret_cast<u32x2*>(wl + r32 * 144 + (j * 32 + 8 * g + 4 * h) * 2) = v;
    }
  WAITL0();
  __builtin_amdgcn_wave_barrier();
#pragma unroll
  for (int i = 0; i < 4; ++i) {
    const int row = i * 8 + (l >> 3), ch = l & 7;
    const u32x4 v = *reinterpret_cast<const u32x4*>(wl + row * 144 + ch * 16);
    const int token = mt * 128 + wm * 32 + row;
    *reinterpret_cast<u32x4*>(out + (size_t)token * 1280 + br * 640 + nt * 128 + wn * 64 + ch * 8) = v;
  }
}

extern "C" void kernel_launch(void* const* d_in, const int* in_sizes, int n_in,
                              void* d_out, int out_size, void* d_ws, size_t ws_size,
                              hipStream_t stream) {
  const u16* xA = (const u16*)d_in[0];
  const u16* wA = (const u16*)d_in[1];
  const u16* woA = (const u16*)d_in[2];
  const u16* xB = (const u16*)d_in[3];
  const u16* wB = (const u16*)d_in[4];
  const u16* woB = (const u16*)d_in[5];
  char* ws = (char*)d_ws;
  (void)hipFuncSetAttribute((const void*)k1_qkv, hipFuncAttributeMaxDynamicSharedMemorySize, G1::LDS_BYTES);
  (void)hipFuncSetAttribute((const void*)k2_attn, hipFuncAttributeMaxDynamicSharedMemorySize, K2_LDS);
  (void)hipFuncSetAttribute((const void*)k3_out, hipFuncAttributeMaxDynamicSharedMemorySize, G3::LDS_BYTES);
  k1_qkv<<<240, 512, G1::LDS_BYTES, stream>>>(xA, wA, xB, wB, ws);
  k2_attn<<<256, 64 * NW, K2_LDS, stream>>>(ws);
  k3_out<<<160, 512, G3::LDS_BYTES, stream>>>(ws, woA, woB, (u16*)d_out);
}
```

```cpp
#include <hip/hip_runtime.h>
#include <hip/hip_bf16.h>

typedef __attribute__((ext_vector_type(8))) short bf16x8;
typedef __attribute__((ext_vector_type(4))) short s16x4;
typedef __attribute__((ext_vector_type(16))) float f32x16;
typedef __attribute__((ext_vector_type(2))) float f32x2;
typedef __attribute__((ext_vector_type(2))) __bf16 bf16x2;
typedef __attribute__((ext_vector_type(4))) unsigned u32x4;
typedef __attribute__((ext_vector_type(2))) unsigned u32x2;
typedef unsigned short u16;

#define DI __device__ __forceinline__
#define MFMA32(a, b, c) __builtin_amdgcn_mfma_f32_32x32x16_bf16((a), (b), (c), 0, 0, 0)
#define WAITV(n) asm volatile("s_waitcnt vmcnt(%0)" ::"n"(n) : "memory")
#define WAITL0() asm volatile("s_waitcnt lgkmcnt(0)" ::: "memory")
#define BAR() do { asm volatile("" ::: "memory"); __builtin_amdgcn_s_barrier(); asm volatile("" ::: "memory"); } while (0)
#define SB0() __builtin_amdgcn_sched_barrier(0)
#define BC8(x) __builtin_bit_cast(bf16x8, (x))

DI void dma16(const char* base, unsigned voff, unsigned ldsaddr) {
  unsigned keep;
  asm volatile("s_mov_b32 %0, m0\n\ts_mov_b32 m0, %3\n\ts_nop 0\n\tglobal_load_lds_dwordx4 %1, %2\n\ts_mov_b32 m0, %0"
               : "=&s"(keep) : "v"(voff), "s"(base), "s"(ldsaddr) : "memory");
}
DI unsigned lds_addr(const char* p) { return (unsigned)(size_t)((const __attribute__((address_space(3))) char*)p); }

constexpr int S = 2048, D = 640, N3 = 1920, H = 32;

constexpr size_t SZ_P = (size_t)160 * S * 8;
constexpr int KV_TILE = 2816;
constexpr size_t SZ_KV = (size_t)H * 64 * KV_TILE;
constexpr size_t WS_QB = 0;
constexpr size_t WS_KVB = WS_QB + SZ_P;
constexpr size_t WS_QA = WS_KVB + SZ_KV;
constexpr size_t WS_O = WS_QA + 3 * SZ_P;

DI unsigned pk2(float lo, float hi) {
  f32x2 v = {lo, hi};
  return __builtin_bit_cast(unsigned, __builtin_convertvector(v, bf16x2));
}
DI float bf_lo(unsigned u) { return __uint_as_float(u << 16); }
DI float bf_hi(unsigned u) { return __uint_as_float(u & 0xffff0000u); }
DI float rbf(float x) { return bf_lo(pk2(x, 0.f)); }

DI bf16x8 load_bfrag(const char* lb, int krow0, int col0, int l) {
  typedef __attribute__((address_space(3))) s16x4* lp4;
  const int h = l >> 5, i16 = l & 15, q = i16 >> 2, p = i16 & 3, blk = (l >> 4) & 1;
  const int c0 = col0 / 8 + blk * 2 + (p >> 1);
  const int k0 = krow0 + 8 * h + q, k1 = k0 + 4;
  const int f0 = ((k0 & 3) << 2) | ((k0 >> 2) & 3), f1 = ((k1 & 3) << 2) | ((k1 >> 2) & 3);
  const s16x4 lo = __builtin_amdgcn_ds_read_tr16_b64_v4i16((lp4)(lb + k0 * 256 + ((c0 ^ f0) << 4) + 8 * (p & 1)));
  const s16x4 hi = __builtin_amdgcn_ds_read_tr16_b64_v4i16((lp4)(lb + k1 * 256 + ((c0 ^ f1) << 4) + 8 * (p & 1)));
  return __builtin_shufflevector(lo, hi, 0, 1, 2, 3, 4, 5, 6, 7);
}

constexpr int K1_STAGE = 49152, K1_LDS = 3 * K1_STAGE;
struct Frag1 { bf16x8 a[2], b[2]; };

DI void k1_load_frag(Frag1& f, const char* la, int ks, int wm, int wn, int l) {
  const int r32 = l & 31, h = l >> 5;
#pragma unroll
  for (int i = 0; i < 2; ++i) {
    const int row = wm * 64 + i * 32 + r32;
    f.a[i] = *reinterpret_cast<const bf16x8*>(la + row * 128 + (((2 * ks + h) ^ ((row >> 1) & 7)) << 4));
  }
#pragma unroll
  for (int j = 0; j < 2; ++j) f.b[j] = load_bfrag(la + 32768, 16 * ks, wn * 64 + j * 32, l);
}
template <bool SWAP>
DI void k1_mma(const Frag1& f, f32x16 (&acc)[2][2]) {
#pragma unroll
  for (int i = 0; i < 2; ++i)
#pragma unroll
    for (int j = 0; j < 2; ++j) acc[i][j] = SWAP ? MFMA32(f.b[j], f.a[i], acc[i][j]) : MFMA32(f.a[i], f.b[j], acc[i][j]);
}
template <bool SWAP>
DI void k1_compute(const char* lds, f32x16 (&acc)[2][2], int wm, int wn, int l) {
#pragma unroll
  for (int i = 0; i < 2; ++i)
#pragma unroll
    for (int j = 0; j < 2; ++j)
#pragma unroll
      for (int r = 0; r < 16; ++r) acc[i][j][r] = 0.f;
  Frag1 fa, fb;
#pragma unroll
  for (int i = 0; i < 2; ++i) { fa.a[i] = bf16x8{0, 0, 0, 0, 0, 0, 0, 0}; fa.b[i] = bf16x8{0, 0, 0, 0, 0, 0, 0, 0}; }
  int cur = 0;
#pragma unroll 1
  for (int it = 0; it < 10; ++it) {
    WAITL0();
    BAR();
    const char* la = lds + cur;
    k1_load_frag(fb, la, 0, wm, wn, l); SB0();
    k1_mma<SWAP>(fa, acc); SB0();
    k1_load_frag(fa, la, 1, wm, wn, l); SB0();
    k1_mma<SWAP>(fb, acc); SB0();
    k1_load_frag(fb, la, 2, wm, wn, l); SB0();
    k1_mma<SWAP>(fa, acc); SB0();
    k1_load_frag(fa, la, 3, wm, wn, l); SB0();
    k1_mma<SWAP>(fb, acc); SB0();
    cur = (cur == 2 * K1_STAGE) ? 0 : cur + K1_STAGE;
  }
  k1_mma<SWAP>(fa, acc);
}

DI void k1_issue(const char* a, const char* b, unsigned lbase, int lw, const unsigned (&ao)[2], const unsigned (&bo)[4]) {
  if (lw == 0) {
#pragma unroll
    for (int p = 0; p < 24; ++p) dma16(a + p * 10240, ao[p & 1], lbase + p * 1024);
  } else {
#pragma unroll
    for (int p = 24; p < 32; ++p) dma16(a + p * 10240, ao[p & 1], lbase + p * 1024);
#pragma unroll
    for (int p = 0; p < 16; ++p) dma16(b + p * (4 * N3 * 2), bo[p & 3], lbase + 32768 + p * 1024);
  }
}

__global__ __launch_bounds__(640) void k1_qkv(const u16* __restrict__ xA, const u16* __restrict__ wA,
                                              const u16* __restrict__ xB, const u16* __restrict__ wB, char* __restrict__ ws) {
  extern __shared__ __attribute__((aligned(16))) char lds[];
  const int bid = blockIdx.x;
  const int L = (bid & 7) * 30 + (bid >> 3);
  const int br = L / 120, r = L % 120, mt = r / 15, nt = r % 15;
  const int sec = nt / 5;
  const int tid = threadIdx.x, l = tid & 63, r32 = l & 31, h = l >> 5;
  const int w = __builtin_amdgcn_readfirstlane(tid >> 6);
  if (w >= 8) {
    const char* A = reinterpret_cast<const char*>((br ? xB : xA) + (size_t)mt * 256 * D);
    const char* B = reinterpret_cast<const char*>((br ? wB : wA) + nt * 128);
    unsigned ao[2], bo[4];
#pragma unroll
    for (int e = 0; e < 2; ++e) ao[e] = (l >> 3) * (D * 2) + (((l & 7) ^ (((l >> 4) + 4 * e) & 7)) << 4);
#pragma unroll
    for (int e = 0; e < 4; ++e) bo[e] = (l >> 4) * (N3 * 2) + (((l & 15) ^ (((l >> 4) << 2) | e)) << 4);
    const unsigned lb = lds_addr(lds);
    const int lw = w - 8;
    int k2 = (0x8530 >> ((mt >> 1) * 4)) & 15;
    k1_issue(A + k2 * 128, B + k2 * (64 * N3 * 2), lb, lw, ao, bo); k2 = (k2 == 9) ? 0 : k2 + 1;
    k1_issue(A + k2 * 128, B + k2 * (64 * N3 * 2), lb + K1_STAGE, lw, ao, bo); k2 = (k2 == 9) ? 0 : k2 + 1;
    int nxt = 2 * K1_STAGE;
#pragma unroll 1
    for (int it = 0; it < 8; ++it) {
      WAITV(24);
      BAR();
      k1_issue(A + k2 * 128, B + k2 * (64 * N3 * 2), lb + nxt, lw, ao, bo); k2 = (k2 == 9) ? 0 : k2 + 1;
      nxt = (nxt == 2 * K1_STAGE) ? 0 : nxt + K1_STAGE;
    }
    WAITV(24); BAR();
    WAITV(0); BAR();
    return;
  }
  const int wm = w & 3, wn = w >> 2;
  f32x16 acc[2][2];
  const bool vtype = (br == 1 && sec == 2);
  if (vtype) {
    k1_compute<false>(lds, acc, wm, wn, l);
#pragma unroll
    for (int j = 0; j < 2; ++j) {
      const int c = nt * 128 + wn * 64 + j * 32 + r32 - 1280;
      const int head = c / 20, d = c - head * 20;
#pragma unroll
      for (int i = 0; i < 2; ++i) {
        const int tile = mt * 8 + wm * 2 + i;
        char* base = ws + WS_KVB + ((size_t)head * 64 + tile) * KV_TILE + 1280 + h * 384 + d * 16;
#pragma unroll
        for (int s = 0; s < 2; ++s) {
          u32x4 v = {pk2(acc[i][j][8 * s], acc[i][j][8 * s + 1]), pk2(acc[i][j][8 * s + 2], acc[i][j][8 * s + 3]),
                     pk2(acc[i][j][8 * s + 4], acc[i][j][8 * s + 5]), pk2(acc[i][j][8 * s + 6], acc[i][j][8 * s + 7])};
          *reinterpret_cast<u32x4*>(base + s * 768) = v;
          if (d == 19) { u32x4 one = {0x3f803f80u, 0x3f803f80u, 0x3f803f80u, 0x3f803f80u}; *reinterpret_cast<u32x4*>(base + s * 768 + 16) = one; }
        }
      }
    }
  } else {
    k1_compute<true>(lds, acc, wm, wn, l);
    const bool isKB = (br == 1 && sec == 1), isQB = (br == 1 && sec == 0);
    const float qs = 0.22360679774997896f * 1.4426950408889634f;
    char* pbase = ws + (br ? WS_QB : WS_QA + sec * SZ_P);
#pragma unroll
    for (int i = 0; i < 2; ++i) {
      const int token = mt * 256 + wm * 64 + i * 32 + r32;
#pragma unroll
      for (int j = 0; j < 2; ++j) {
#pragma unroll
        for (int g = 0; g < 4; ++g) {
          const int c4 = (nt * 128 + wn * 64 + j * 32 + 8 * g + 4 * h - sec * 640) >> 2;
          unsigned lo = pk2(acc[i][j][4 * g], acc[i][j][4 * g + 1]), hi = pk2(acc[i][j][4 * g + 2], acc[i][j][4 * g + 3]);
          char* dst;
          if (isKB) {
            const int head = c4 / 5, piece = c4 - head * 5;
            dst = ws + WS_KVB + ((size_t)head * 64 + (token >> 5)) * KV_TILE + piece * 256 + r32 * 8;
          } else {
            if (isQB) { lo = pk2(bf_lo(lo) * qs, bf_hi(lo) * qs); hi = pk2(bf_lo(hi) * qs, bf_hi(hi) * qs); }
            dst = pbase + ((size_t)c4 * S + token) * 8;
          }
          u32x2 v = {lo, hi}; *reinterpret_cast<u32x2*>(dst) = v;
        }
      }
    }
  }
}

#define K2_N0 36
constexpr int N0 = K2_N0, N1 = 64 - K2_N0;
constexpr int K2_LDS = 4 * 2 * 13 * 256;
constexpr float MAX_SLACK = 16.f;
constexpr float L_LIMIT = 281474976710656.f;

DI bf16x8 pack8(const f32x16& x, int s) {
  u32x4 v = {pk2(x[8 * s], x[8 * s + 1]), pk2(x[8 * s + 2], x[8 * s + 3]), pk2(x[8 * s + 4], x[8 * s + 5]), pk2(x[8 * s + 6], x[8 * s + 7])};
  return BC8(v);
}
struct KV { u32x4 k0, k1; bf16x8 v0, v1; };
DI void load_k(KV& f, const char* tb, unsigned koff) {
  const u32x2 a = *reinterpret_cast<const u32x2*>(tb + koff);
  const u32x2 b = *reinterpret_cast<const u32x2*>(tb + koff + 256);
  const u32x2 c = *reinterpret_cast<const u32x2*>(tb + (koff & 255) + 1024);
  f.k0 = u32x4{a[0], a[1], b[0], b[1]}; f.k1[0] = c[0]; f.k1[1] = c[1];
}
DI void load_v(KV& f, const char* tb, unsigned voff) {
  f.v0 = *reinterpret_cast<const bf16x8*>(tb + voff); f.v1 = *reinterpret_cast<const bf16x8*>(tb + voff + 768);
}
DI f32x16 zero16() { f32x16 z;
#pragma unroll
  for (int r = 0; r < 16; ++r) z[r] = 0.f;
  return z; }
DI f32x16 qk(const KV& k, const u32x4& q0, const u32x4& q1) {
  f32x16 s = MFMA32(BC8(k.k0), BC8(q0), zero16());
  return MFMA32(BC8(k.k1), BC8(q1), s);
}
template <int A, int B> DI void exps(f32x16& s) {
#pragma unroll
  for (int r = A; r < B; ++r) s[r] = __builtin_amdgcn_exp2f(s[r]);
}

DI void local_attn(char* __restrict__ ws, int head, int token) {
  const char* qb = ws + WS_QA + (size_t)head * 5 * S * 8;
  const char* kb = qb + SZ_P;
  const char* vb = qb + 2 * SZ_P;
  float q[20];
#pragma unroll
  for (int i = 0; i < 5; ++i) {
    const u32x2 u = *reinterpret_cast<const u32x2*>(qb + ((size_t)i * S + token) * 8);
    q[4 * i] = bf_lo(u[0]); q[4 * i + 1] = bf_hi(u[0]); q[4 * i + 2] = bf_lo(u[1]); q[4 * i + 3] = bf_hi(u[1]);
  }
  float lg[8];
#pragma unroll
  for (int wdx = 0; wdx < 8; ++wdx) {
    const int t = token - 2 * wdx, tc = t < 0 ? 0 : t;
    float a = 0.f;
#pragma unroll
    for (int i = 0; i < 5; ++i) {
      const u32x2 u = *reinterpret_cast<const u32x2*>(kb + ((size_t)i * S + tc) * 8);
      a = fmaf(q[4 * i], bf_lo(u[0]), a); a = fmaf(q[4 * i + 1], bf_hi(u[0]), a);
      a = fmaf(q[4 * i + 2], bf_lo(u[1]), a); a = fmaf(q[4 * i + 3], bf_hi(u[1]), a);
    }
    a = rbf(rbf(a) * 0.22360679774997896f);
    lg[wdx] = (t < 0) ? rbf(-1e9f) : a;
  }
  float m = lg[0];
#pragma unroll
  for (int i = 1; i < 8; ++i) m = fmaxf(m, lg[i]);
  float sum = 0.f;
#pragma unroll
  for (int i = 0; i < 8; ++i) { lg[i] = __builtin_amdgcn_exp2f((lg[i] - m) * 1.4426950408889634f); sum += lg[i]; }
  const float inv = 1.0f / sum;
  float o[20];
#pragma unroll
  for (int i = 0; i < 20; ++i) o[i] = 0.f;
#pragma unroll
  for (int wdx = 0; wdx < 8; ++wdx) {
    const int t = token - 2 * wdx, tc = t < 0 ? 0 : t;
    const float pw = rbf(lg[wdx] * inv);
#pragma unroll
    for (int i = 0; i < 5; ++i) {
      const u32x2 u = *reinterpret_cast<const u32x2*>(vb + ((size_t)i * S + tc) * 8);
      o[4 * i] = fmaf(pw, bf_lo(u[0]), o[4 * i]); o[4 * i + 1] = fmaf(pw, bf_hi(u[0]), o[4 * i + 1]);
      o[4 * i + 2] = fmaf(pw, bf_lo(u[1]), o[4 * i + 2]); o[4 * i + 3] = fmaf(pw, bf_hi(u[1]), o[4 * i + 3]);
    }
  }
  char* ob = ws + WS_O + ((size_t)head * 5 * S + token) * 8;
#pragma unroll
  for (int i = 0; i < 5; ++i) {
    u32x2 u = {pk2(o[4 * i], o[4 * i + 1]), pk2(o[4 * i + 2], o[4 * i + 3])};
    *reinterpret_cast<u32x2*>(ob + (size_t)i * S * 8) = u;
  }
}

DI void exact_attn(const char* kv, unsigned koff, unsigned voff, const u32x4& q0, u32x4 q1, int h, f32x16& o) {
  if (h == 0) q1[2] = 0u;
  float m = -3.0e38f;
  o = zero16();
  KV f; f.k1[2] = 0x00003f80u; f.k1[3] = 0u;
#pragma unroll 1
  for (int t = 0; t < 64; ++t) {
    load_k(f, kv + t * KV_TILE, koff); load_v(f, kv + t * KV_TILE, voff);
    f32x16 s = qk(f, q0, q1);
    float tm = s[0];
#pragma unroll
    for (int r = 1; r < 16; ++r) tm = fmaxf(tm, s[r]);
    tm = fmaxf(tm, __shfl_xor(tm, 32));
    const float mn = fmaxf(m, tm);
    const float alpha = __builtin_amdgcn_exp2f(m - mn);
    m = mn;
#pragma unroll
    for (int r = 0; r < 16; ++r) { o[r] *= alpha; s[r] = __builtin_amdgcn_exp2f(s[r] - mn); }
    o = MFMA32(f.v0, pack8(s, 0), o);
    o = MFMA32(f.v1, pack8(s, 1), o);
  }
}

DI float first_max(const f32x16& s) {
  float m = s[0];
#pragma unroll
  for (int r = 1; r < 16; ++r) m = fmaxf(m, s[r]);
  return rbf(fmaxf(m, __shfl_xor(m, 32)) + MAX_SLACK);
}

#define K2_STEP(cur, nx, ld, tld)                                               \
  do {                                                                            \
    const char* tb_ = kv + ((tld) & 63) * KV_TILE;                                \
    exps<0, 8>(sa); SB0();                                                        \
    { const bf16x8 p_ = pack8(sa, 0); oa = MFMA32(cur.v0, p_, oa); } SB0();       \
    exps<8, 16>(sa); SB0();                                                       \
    { const bf16x8 p_ = pack8(sa, 1); oa = MFMA32(cur.v1, p_, oa); } SB0();       \
    exps<0, 4>(sb); SB0();                                                        \
    sa = MFMA32(BC8(nx.k0), BC8(qa0), zero16()); SB0();                           \
    exps<4, 8>(sb); SB0();                                                        \
    sa = MFMA32(BC8(nx.k1), BC8(qa1), sa); SB0();                                 \
    { const bf16x8 p_ = pack8(sb, 0); ob = MFMA32(cur.v0, p_, ob); } SB0();       \
    exps<8, 16>(sb); SB0();                                                       \
    { const bf16x8 p_ = pack8(sb, 1); ob = MFMA32(cur.v1, p_, ob); } SB0();       \
    load_k(ld, tb_, koff); load_v(cur, kv + (((tld) + 1) & 63) * KV_TILE, voff); SB0();               \
    sb = MFMA32(BC8(nx.k0), BC8(qb0), zero16()); SB0();                           \
    sb = MFMA32(BC8(nx.k1), BC8(qb1), sb); SB0();                                 \
  } while (0)

__global__ __launch_bounds__(512) void k2_attn(char* __restrict__ ws) {
  extern __shared__ __attribute__((aligned(16))) char lds[];
  const int bid = blockIdx.x;
  const int head = (bid & 7) * 4 + ((bid >> 3) & 3), qt = bid >> 5;
  const int tid = threadIdx.x, l = tid & 63, r32 = l & 31, h = l >> 5;
  const int w = __builtin_amdgcn_readfirstlane(tid >> 6);
  const int qp = w & 3, kg = w >> 2;
  const char* kv = ws + WS_KVB + (size_t)head * 64 * KV_TILE;
  const int t0 = qt * 256;
  const unsigned koff = (2 * h) * 256 + r32 * 8, voff = 1280 + h * 384 + (r32 < 20 ? r32 : 20) * 16;
  const int tstart = qt * 8 + (kg ? N0 : 0);
  const int nsteps = kg ? N1 : N0;

  KV f0, f1, f2, f3;
  f0.k1[2] = f1.k1[2] = f2.k1[2] = f3.k1[2] = 0x00003f80u;
  f0.k1[3] = f1.k1[3] = f2.k1[3] = f3.k1[3] = 0u;
  u32x4 qa0, qa1, qb0, qb1;
  {
    const char* qp_ = ws + WS_QB + ((size_t)head * 5 * S + t0 + qp * 64 + r32) * 8;
    const u32x2 a0 = *reinterpret_cast<const u32x2*>(qp_ + (size_t)(2 * h) * S * 8), b0 = *reinterpret_cast<const u32x2*>(qp_ + (size_t)(2 * h + 1) * S * 8);
    const u32x2 c0 = *reinterpret_cast<const u32x2*>(qp_ + (size_t)4 * S * 8);
    const u32x2 a1 = *reinterpret_cast<const u32x2*>(qp_ + 256 + (size_t)(2 * h) * S * 8), b1 = *reinterpret_cast<const u32x2*>(qp_ + 256 + (size_t)(2 * h + 1) * S * 8);
    const u32x2 c1 = *reinterpret_cast<const u32x2*>(qp_ + 256 + (size_t)4 * S * 8);
    qa0 = u32x4{a0[0], a0[1], b0[0], b0[1]}; qa1 = h ? u32x4{0u, 0u, 0u, 0u} : u32x4{c0[0], c0[1], 0u, 0u};
    qb0 = u32x4{a1[0], a1[1], b1[0], b1[1]}; qb1 = h ? u32x4{0u, 0u, 0u, 0u} : u32x4{c1[0], c1[1], 0u, 0u};
  }
  load_k(f0, kv + (tstart & 63) * KV_TILE, koff); load_v(f0, kv + (tstart & 63) * KV_TILE, voff);
  load_k(f1, kv + ((tstart + 1) & 63) * KV_TILE, koff); load_v(f1, kv + ((tstart + 1) & 63) * KV_TILE, voff);
  load_k(f2, kv + ((tstart + 2) & 63) * KV_TILE, koff); load_v(f2, kv + ((tstart + 2) & 63) * KV_TILE, voff);
  load_v(f3, kv + ((tstart + 3) & 63) * KV_TILE, voff);

  if (kg) local_attn(ws, head, t0 + tid - 256);

  const float ma = first_max(qk(f0, qa0, qa1)), mb = first_max(qk(f0, qb0, qb1));
  if (h == 0) { qa1[2] = pk2(-ma, 0.f); qb1[2] = pk2(-mb, 0.f); }
  f32x16 oa = zero16(), ob = zero16();
  f32x16 sa = qk(f0, qa0, qa1), sb = qk(f0, qb0, qb1);

#pragma unroll 1
  for (int t = tstart; t < tstart + nsteps; t += 4) {
    K2_STEP(f0, f1, f3, t + 3);
    K2_STEP(f1, f2, f0, t + 4);
    K2_STEP(f2, f3, f1, t + 5);
    K2_STEP(f3, f0, f2, t + 6);
  }

  float* xb = reinterpret_cast<float*>(lds) + qp * (2 * 13 * 64) + l;
  if (kg) {
#pragma unroll
    for (int r = 0; r < 12; ++r) { xb[r * 64] = oa[r]; xb[(13 + r) * 64] = ob[r]; }
    xb[12 * 64] = ma; xb[25 * 64] = mb;
  }
  __syncthreads();
  if (kg) return;
  {
    const float fa = __builtin_amdgcn_exp2f(xb[12 * 64] - ma), fb = __builtin_amdgcn_exp2f(xb[25 * 64] - mb);
#pragma unroll
    for (int r = 0; r < 12; ++r) { oa[r] = fmaf(xb[r * 64], fa, oa[r]); ob[r] = fmaf(xb[(13 + r) * 64], fb, ob[r]); }
  }
  float la = __shfl_xor(oa[8], 32), lb = __shfl_xor(ob[8], 32);
  if (h) { la = oa[8]; lb = ob[8]; }
  if (__any(!(la < L_LIMIT))) { exact_attn(kv, koff, voff, qa0, qa1, h, oa); la = __shfl_xor(oa[8], 32); if (h) la = oa[8]; }
  if (__any(!(lb < L_LIMIT))) { exact_attn(kv, koff, voff, qb0, qb1, h, ob); lb = __shfl_xor(ob[8], 32); if (h) lb = ob[8]; }
  const float ia = 1.0f / la, ib = 1.0f / lb;
  char* op = ws + WS_O + SZ_P + ((size_t)(head * 5 + h) * S + t0 + qp * 64 + r32) * 8;
  { u32x2 u = {pk2(oa[0] * ia, oa[1] * ia), pk2(oa[2] * ia, oa[3] * ia)}; *reinterpret_cast<u32x2*>(op) = u; }
  { u32x2 u = {pk2(oa[4] * ia, oa[5] * ia), pk2(oa[6] * ia, oa[7] * ia)}; *reinterpret_cast<u32x2*>(op + (size_t)2 * S * 8) = u; }
  { u32x2 u = {pk2(ob[0] * ib, ob[1] * ib), pk2(ob[2] * ib, ob[3] * ib)}; *reinterpret_cast<u32x2*>(op + 256) = u; }
  { u32x2 u = {pk2(ob[4] * ib, ob[5] * ib), pk2(ob[6] * ib, ob[7] * ib)}; *reinterpret_cast<u32x2*>(op + 256 + (size_t)2 * S * 8) = u; }
  if (h == 0) {
    { u32x2 u = {pk2(oa[8] * ia, oa[9] * ia), pk2(oa[10] * ia, oa[11] * ia)}; *reinterpret_cast<u32x2*>(op + (size_t)4 * S * 8) = u; }
    { u32x2 u = {pk2(ob[8] * ib, ob[9] * ib), pk2(ob[10] * ib, ob[11] * ib)}; *reinterpret_cast<u32x2*>(op + 256 + (size_t)4 * S * 8) = u; }
  }
}

constexpr int K3_LDS = 163840;

__global__ __launch_bounds__(512) void k3_out(const char* __restrict__ ws, const u16* __restrict__ woA, const u16* __restrict__ woB,
                                              u16* __restrict__ out) {
  extern __shared__ __attribute__((aligned(16))) char lds[];
  const int bid = blockIdx.x;
  const int L = (bid & 7) * 20 + (bid >> 3);
  const int br = L / 80, r = L % 80, mt = r / 5, nt = r % 5;
  const int tid = threadIdx.x, l = tid & 63, r32 = l & 31, h = l >> 5;
  const int w = __builtin_amdgcn_readfirstlane(tid >> 6);
  const int wm = w & 3, wn = w >> 2;
  const char* A = ws + WS_O + br * SZ_P + ((size_t)(2 * h) * S + mt * 128 + wm * 32 + r32) * 8;
  const char* B = reinterpret_cast<const char*>((br ? woB : woA) + nt * 128);
  const unsigned lb = lds_addr(lds);
  const unsigned bo = (l >> 4) * (D * 2) + (((l & 15) ^ (((l >> 4) << 2) | (w & 3))) << 4);
  u32x4 af[40];
#pragma unroll
  for (int half = 0; half < 2; ++half) {
#pragma unroll
    for (int ks = half * 20; ks < half * 20 + 20; ++ks) {
      const u32x2 lo = *reinterpret_cast<const u32x2*>(A + (size_t)(4 * ks) * S * 8);
      const u32x2 hi = *reinterpret_cast<const u32x2*>(A + (size_t)(4 * ks + 1) * S * 8);
      af[ks] = u32x4{lo[0], lo[1], hi[0], hi[1]};
    }
    asm volatile("" ::: "memory");
#pragma unroll
    for (int i = half * 10; i < half * 10 + 10; ++i) dma16(B + (size_t)(i * 8 + w) * (4 * D * 2), bo, lb + (i * 8 + w) * 1024);
  }
  f32x16 acc[2] = {zero16(), zero16()};
#pragma unroll
  for (int half = 0; half < 2; ++half) {
    if (half == 0) WAITV(50); else WAITV(0);
    BAR();
    bf16x8 b0 = load_bfrag(lds, half * 320, wn * 64, l), b1 = load_bfrag(lds, half * 320, wn * 64 + 32, l);
#pragma unroll
    for (int ks = half * 20; ks < half * 20 + 20; ++ks) {
      bf16x8 n0 = b0, n1 = b1;
      if (ks + 1 < half * 20 + 20) { n0 = load_bfrag(lds, (ks + 1) * 16, wn * 64, l); n1 = load_bfrag(lds, (ks + 1) * 16, wn * 64 + 32, l); }
      acc[0] = MFMA32(b0, BC8(af[ks]), acc[0]);
      acc[1] = MFMA32(b1, BC8(af[ks]), acc[1]);
      b0 = n0; b1 = n1;
    }
  }
  WAITL0();
  BAR();
  char* wl = lds + w * (32 * 144);
#pragma unroll
  for (int j = 0; j < 2; ++j)
#pragma unroll
    for (int g = 0; g < 4; ++g) {
      u32x2 v = {pk2(acc[j][4 * g], acc[j][4 * g + 1]), pk2(acc[j][4 * g + 2], acc[j][4 * g + 3])};
      *reinterpret_cast<u32x2*>(wl + r32 * 144 + (j * 32 + 8 * g + 4 * h) * 2) = v;
    }
  WAITL0();
  __builtin_amdgcn_wave_barrier();
#pragma unroll
  for (int i = 0; i < 4; ++i) {
    const int row = i * 8 + (l >> 3), ch = l & 7;
    const u32x4 v = *reinterpret_cast<const u32x4*>(wl + row * 144 + ch * 16);
    const int token = mt * 128 + wm * 32 + row;
    *reinterpret_cast<u32x4*>(out + (size_t)token * 1280 + br * 640 + nt * 128 + wn * 64 + ch * 8) = v;
  }
}

extern "C" void kernel_launch(void* const* d_in, const int* in_sizes, int n_in,
                              void* d_out, int out_size, void* d_ws, size_t ws_size,
                              hipStream_t stream) {
  const u16* xA = (const u16*)d_in[0];
  const u16* wA = (const u16*)d_in[1];
  const u16* woA = (const u16*)d_in[2];
  const u16* xB = (const u16*)d_in[3];
  const u16* wB = (const u16*)d_in[4];
  const u16* woB = (const u16*)d_in[5];
  char* ws = (char*)d_ws;
  (void)hipFuncSetAttribute((const void*)k1_qkv, hipFuncAttributeMaxDynamicSharedMemorySize, K1_LDS);
  (void)hipFuncSetAttribute((const void*)k3_out, hipFuncAttributeMaxDynamicSharedMemorySize, K3_LDS);
  k1_qkv<<<240, 640, K1_LDS, stream>>>(xA, wA, xB, wB, ws);
  k2_attn<<<256, 512, K2_LDS, stream>>>(ws);
  k3_out<<<160, 512, K3_LDS, stream>>>(ws, woA, woB, (u16*)d_out);
}
```

```cpp
#include <hip/hip_runtime.h>
#include <hip/hip_bf16.h>

typedef __attribute__((ext_vector_type(8))) short bf16x8;
typedef __attribute__((ext_vector_type(4))) short s16x4;
typedef __attribute__((ext_vector_type(16))) float f32x16;
typedef __attribute__((ext_vector_type(2))) float f32x2;
typedef __attribute__((ext_vector_type(2))) __bf16 bf16x2;
typedef __attribute__((ext_vector_type(4))) unsigned u32x4;
typedef __attribute__((ext_vector_type(2))) unsigned u32x2;
typedef unsigned short u16;

#define DI __device__ __forceinline__
#define MFMA32(a, b, c) __builtin_amdgcn_mfma_f32_32x32x16_bf16((a), (b), (c), 0, 0, 0)
#define WAITV(n) asm volatile("s_waitcnt vmcnt(%0)" ::"n"(n) : "memory")
#define WAITL0() asm volatile("s_waitcnt lgkmcnt(0)" ::: "memory")
#define BAR() do { asm volatile("" ::: "memory"); __builtin_amdgcn_s_barrier(); asm volatile("" ::: "memory"); } while (0)
#define SB0() __builtin_amdgcn_sched_barrier(0)
#define BC8(x) __builtin_bit_cast(bf16x8, (x))

DI void dma16(const char* base, unsigned voff, unsigned ldsaddr) {
  unsigned keep;
  asm volatile("s_mov_b32 %0, m0\n\ts_mov_b32 m0, %3\n\ts_nop 0\n\tglobal_load_lds_dwordx4 %1, %2\n\ts_mov_b32 m0, %0"
               : "=&s"(keep) : "v"(voff), "s"(base), "s"(ldsaddr) : "memory");
}
DI unsigned lds_addr(const char* p) { return (unsigned)(size_t)((const __attribute__((address_space(3))) char*)p); }

constexpr int S = 2048, D = 640, N3 = 1920, H = 32;

constexpr int PSTR = S * 8 + 256;
constexpr size_t SZ_P = (size_t)160 * PSTR;
constexpr int KV_TILE = 3072;
constexpr size_t SZ_KV = (size_t)H * 64 * KV_TILE;
constexpr size_t WS_QB = 0;
constexpr size_t WS_KVB = WS_QB + SZ_P;
constexpr size_t WS_QA = WS_KVB + SZ_KV;
constexpr size_t WS_O = WS_QA + 3 * SZ_P;

DI unsigned pk2(float lo, float hi) {
  f32x2 v = {lo, hi};
  return __builtin_bit_cast(unsigned, __builtin_convertvector(v, bf16x2));
}
DI float bf_lo(unsigned u) { return __uint_as_float(u << 16); }
DI float bf_hi(unsigned u) { return __uint_as_float(u & 0xffff0000u); }
DI float rbf(float x) { return bf_lo(pk2(x, 0.f)); }

DI bf16x8 load_bfrag(const char* lb, int krow0, int col0, int l) {
  typedef __attribute__((address_space(3))) s16x4* lp4;
  const int h = l >> 5, i16 = l & 15, q = i16 >> 2, p = i16 & 3, blk = (l >> 4) & 1;
  const int c0 = col0 / 8 + blk * 2 + (p >> 1);
  const int k0 = krow0 + 8 * h + q, k1 = k0 + 4;
  const int f0 = ((k0 & 3) << 2) | ((k0 >> 2) & 3), f1 = ((k1 & 3) << 2) | ((k1 >> 2) & 3);
  const s16x4 lo = __builtin_amdgcn_ds_read_tr16_b64_v4i16((lp4)(lb + k0 * 256 + ((c0 ^ f0) << 4) + 8 * (p & 1)));
  const s16x4 hi = __builtin_amdgcn_ds_read_tr16_b64_v4i16((lp4)(lb + k1 * 256 + ((c0 ^ f1) << 4) + 8 * (p & 1)));
  return __builtin_shufflevector(lo, hi, 0, 1, 2, 3, 4, 5, 6, 7);
}

#define K1_TOUCH 0
constexpr int K1_STAGE = 49152, K1_LDS = 3 * K1_STAGE + 256, K1_THREADS = 768 + 64 * K1_TOUCH;
struct Frag1 { bf16x8 a[2], b[2]; };

template <int AMODE, int BM>
DI bf16x8 g_load_a(const char* la, int ks, int row, int h) {
  if constexpr (AMODE == 0) {
    return *reinterpret_cast<const bf16x8*>(la + row * 128 + (((2 * ks + h) ^ ((row >> 1) & 7)) << 4));
  } else {
    const s16x4 lo = *reinterpret_cast<const s16x4*>(la + ((4 * ks + 2 * h) * BM + row) * 8);
    const s16x4 hi = *reinterpret_cast<const s16x4*>(la + ((4 * ks + 2 * h + 1) * BM + row) * 8);
    return __builtin_shufflevector(lo, hi, 0, 1, 2, 3, 4, 5, 6, 7);
  }
}
#define GM(i, j) acc[i][j] = SWAP ? MFMA32(u.b[j], u.a[i], acc[i][j]) : MFMA32(u.a[i], u.b[j], acc[i][j])
template <bool SWAP>
DI void g_mma(const Frag1& u, f32x16 (&acc)[2][2]) { GM(0, 0); GM(0, 1); GM(1, 0); GM(1, 1); }
template <bool SWAP, int AMODE, int BM>
DI void g_step(const Frag1& u, f32x16 (&acc)[2][2], Frag1& f, const char* la, int ks, int wm, int wn, int l) {
  const int r32 = l & 31, h = l >> 5;
  GM(0, 0); SB0();
  f.a[0] = g_load_a<AMODE, BM>(la, ks, wm * 64 + r32, h); SB0();
  GM(0, 1); SB0();
  f.b[0] = load_bfrag(la + BM * 128, 16 * ks, wn * 64, l); SB0();
  GM(1, 0); SB0();
  f.a[1] = g_load_a<AMODE, BM>(la, ks, wm * 64 + 32 + r32, h); SB0();
  GM(1, 1); SB0();
  f.b[1] = load_bfrag(la + BM * 128, 16 * ks, wn * 64 + 32, l); SB0();
}
template <bool SWAP, int AMODE, int BM, int NSTAGE>
DI void g_compute(const char* lds, f32x16 (&acc)[2][2], int wm, int wn, int l) {
  constexpr int STAGE = BM * 128 + 16384;
#pragma unroll
  for (int i = 0; i < 2; ++i)
#pragma unroll
    for (int j = 0; j < 2; ++j)
#pragma unroll
      for (int r = 0; r < 16; ++r) acc[i][j][r] = 0.f;
  Frag1 f0, f1, f2, f3;
#pragma unroll
  for (int i = 0; i < 2; ++i) { f2.a[i] = f3.a[i] = bf16x8{0, 0, 0, 0, 0, 0, 0, 0}; f2.b[i] = f3.b[i] = bf16x8{0, 0, 0, 0, 0, 0, 0, 0}; }
  int cur = 0;
#pragma unroll 1
  for (int it = 0; it < 10; ++it) {
    WAITL0();
    BAR();
    const char* la = lds + cur;
    g_step<SWAP, AMODE, BM>(f2, acc, f0, la, 0, wm, wn, l);
    g_step<SWAP, AMODE, BM>(f3, acc, f1, la, 1, wm, wn, l);
    g_step<SWAP, AMODE, BM>(f0, acc, f2, la, 2, wm, wn, l);
    g_step<SWAP, AMODE, BM>(f1, acc, f3, la, 3, wm, wn, l);
    cur = (cur == (NSTAGE - 1) * STAGE) ? 0 : cur + STAGE;
  }
  g_mma<SWAP>(f2, acc);
  g_mma<SWAP>(f3, acc);
}

DI void k1_issue(const char* a, const char* b, unsigned lbase, int lw, const unsigned (&ao)[2], const unsigned (&bo)[4]) {
#pragma unroll
  for (int p = 0; p < 4; ++p) dma16(b + (lw * 4 + p) * (4 * N3 * 2), bo[p], lbase + 32768 + (lw * 4 + p) * 1024);
#pragma unroll
  for (int p = 0; p < 8; ++p) dma16(a + (lw * 8 + p) * 10240, ao[p & 1], lbase + (lw * 8 + p) * 1024);
}

__global__ __launch_bounds__(K1_THREADS) void k1_qkv(const u16* __restrict__ xA, const u16* __restrict__ wA,
                                              const u16* __restrict__ xB, const u16* __restrict__ wB, char* __restrict__ ws) {
  extern __shared__ __attribute__((aligned(16))) char lds[];
  const int bid = blockIdx.x;
  const int L = (bid & 7) * 30 + (bid >> 3);
  const int br = L / 120, r = L % 120, mt = r / 15, nt = r % 15;
  const int sec = nt / 5;
  const int tid = threadIdx.x, l = tid & 63, r32 = l & 31, h = l >> 5;
  const int w = __builtin_amdgcn_readfirstlane(tid >> 6);
  if (K1_TOUCH && w == 12) {
    const char* xb = reinterpret_cast<const char*>(br ? xB : xA) + (size_t)(mt >> 1) * 512 * 1280;
    const char* wb = reinterpret_cast<const char*>(br ? wB : wA);
#pragma unroll
    for (int i = 0; i < 13; ++i) {
      int g = ((bid >> 3) * 13 + i) * 64 + l;
      g = g < 24320 ? g : 24319;
      const char* p = g < 5120 ? xb + (size_t)g * 128 : wb + (size_t)(g - 5120) * 128;
      __builtin_amdgcn_global_load_lds((const __attribute__((address_space(1))) unsigned*)p, (__attribute__((address_space(3))) unsigned*)(lds + 3 * K1_STAGE), 4, 0, 0);
    }
    return;
  }
  if (w >= 8) {
    const char* A = reinterpret_cast<const char*>((br ? xB : xA) + (size_t)mt * 256 * D);
    const char* B = reinterpret_cast<const char*>((br ? wB : wA) + nt * 128);
    unsigned ao[2], bo[4];
#pragma unroll
    for (int e = 0; e < 2; ++e) ao[e] = (l >> 3) * (D * 2) + (((l & 7) ^ (((l >> 4) + 4 * e) & 7)) << 4);
#pragma unroll
    for (int e = 0; e < 4; ++e) bo[e] = (l >> 4) * (N3 * 2) + (((l & 15) ^ (((l >> 4) << 2) | e)) << 4);
    const unsigned lb = lds_addr(lds);
    const int lw = w - 8;
    int k2 = (0x8530 >> ((mt >> 1) * 4)) & 15;
    k1_issue(A + k2 * 128, B + k2 * (64 * N3 * 2), lb, lw, ao, bo); k2 = (k2 == 9) ? 0 : k2 + 1;
    k1_issue(A + k2 * 128, B + k2 * (64 * N3 * 2), lb + K1_STAGE, lw, ao, bo); k2 = (k2 == 9) ? 0 : k2 + 1;
    int nxt = 2 * K1_STAGE;
#pragma unroll 1
    for (int it = 0; it < 8; ++it) {
      WAITV(12);
      BAR();
      k1_issue(A + k2 * 128, B + k2 * (64 * N3 * 2), lb + nxt, lw, ao, bo); k2 = (k2 == 9) ? 0 : k2 + 1;
      nxt = (nxt == 2 * K1_STAGE) ? 0 : nxt + K1_STAGE;
    }
    WAITV(12); BAR();
    WAITV(0); BAR();
    return;
  }
  const int wm = w & 3, wn = w >> 2;
  f32x16 acc[2][2];
  const bool vtype = (br == 1 && sec == 2);
  if (vtype) {
    g_compute<false, 0, 256, 3>(lds, acc, wm, wn, l);
#pragma unroll
    for (int j = 0; j < 2; ++j) {
      const int c = nt * 128 + wn * 64 + j * 32 + r32 - 1280;
      const int head = c / 20, d = c - head * 20;
#pragma unroll
      for (int i = 0; i < 2; ++i) {
        const int tile = mt * 8 + wm * 2 + i;
        char* base = ws + WS_KVB + ((size_t)head * 64 + tile) * KV_TILE + 1536 + h * 384 + d * 16;
#pragma unroll
        for (int s = 0; s < 2; ++s) {
          u32x4 v = {pk2(acc[i][j][8 * s], acc[i][j][8 * s + 1]), pk2(acc[i][j][8 * s + 2], acc[i][j][8 * s + 3]),
                     pk2(acc[i][j][8 * s + 4], acc[i][j][8 * s + 5]), pk2(acc[i][j][8 * s + 6], acc[i][j][8 * s + 7])};
          *reinterpret_cast<u32x4*>(base + s * 768) = v;
          if (d == 19) { u32x4 one = {0x3f803f80u, 0x3f803f80u, 0x3f803f80u, 0x3f803f80u}; *reinterpret_cast<u32x4*>(base + s * 768 + 16) = one; }
        }
      }
    }
  } else {
    g_compute<true, 0, 256, 3>(lds, acc, wm, wn, l);
    const bool isKB = (br == 1 && sec == 1), isQB = (br == 1 && sec == 0);
    const float qs = 0.22360679774997896f * 1.4426950408889634f;
    char* pbase = ws + (br ? WS_QB : WS_QA + sec * SZ_P);
#pragma unroll
    for (int i = 0; i < 2; ++i) {
      const int token = mt * 256 + wm * 64 + i * 32 + r32;
#pragma unroll
      for (int j = 0; j < 2; ++j) {
#pragma unroll
        for (int g = 0; g < 4; ++g) {
          const int c4 = (nt * 128 + wn * 64 + j * 32 + 8 * g + 4 * h - sec * 640) >> 2;
          unsigned lo = pk2(acc[i][j][4 * g], acc[i][j][4 * g + 1]), hi = pk2(acc[i][j][4 * g + 2], acc[i][j][4 * g + 3]);
          if (isKB) {
            const int head = c4 / 5, piece = c4 - head * 5;
            char* dst = ws + WS_KVB + ((size_t)head * 64 + (token >> 5)) * KV_TILE + (piece >> 1) * 512 + r32 * 16 + (piece & 1) * 8;
            if (piece == 4) { u32x4 v = {lo, hi, 0x00003f80u, 0u}; *reinterpret_cast<u32x4*>(dst) = v; }
            else { u32x2 v = {lo, hi}; *reinterpret_cast<u32x2*>(dst) = v; }
          } else {
            if (isQB) { lo = pk2(bf_lo(lo) * qs, bf_hi(lo) * qs); hi = pk2(bf_lo(hi) * qs, bf_hi(hi) * qs); }
            u32x2 v = {lo, hi}; *reinterpret_cast<u32x2*>(pbase + (size_t)c4 * PSTR + token * 8) = v;
          }
        }
      }
    }
  }
}

#define K2_N0 32
constexpr int N0 = K2_N0, N1 = 64 - K2_N0;
constexpr int K2_CMB = 4 * 2 * 13 * 256;
constexpr int K2_LDS = K2_CMB + 4 * 6400;
constexpr float MAX_SLACK = 16.f;
constexpr float L_LIMIT = 281474976710656.f;

DI bf16x8 pack8(const f32x16& x, int s) {
  u32x4 v = {pk2(x[8 * s], x[8 * s + 1]), pk2(x[8 * s + 2], x[8 * s + 3]), pk2(x[8 * s + 4], x[8 * s + 5]), pk2(x[8 * s + 6], x[8 * s + 7])};
  return BC8(v);
}
struct KV { u32x4 k0, k1; bf16x8 v0, v1; };
typedef __amdgpu_buffer_rsrc_t rsrc_t;
DI void load_k(KV& f, rsrc_t kv, int tile, unsigned koff, unsigned koff1) {
  f.k0 = __builtin_amdgcn_raw_buffer_load_b128(kv, koff, tile * KV_TILE, 0);
  f.k1 = __builtin_amdgcn_raw_buffer_load_b128(kv, koff1, tile * KV_TILE, 0);
}
DI void load_v(KV& f, rsrc_t kv, int tile, unsigned voff) {
  f.v0 = BC8(__builtin_amdgcn_raw_buffer_load_b128(kv, voff, tile * KV_TILE, 0));
  f.v1 = BC8(__builtin_amdgcn_raw_buffer_load_b128(kv, voff + 768, tile * KV_TILE, 0));
}
DI f32x16 zero16() { f32x16 z;
#pragma unroll
  for (int r = 0; r < 16; ++r) z[r] = 0.f;
  return z; }
DI f32x16 qk(const KV& k, const u32x4& q0, const u32x4& q1) {
  f32x16 s = MFMA32(BC8(k.k0), BC8(q0), zero16());
  return MFMA32(BC8(k.k1), BC8(q1), s);
}
template <int A, int B> DI void exps(f32x16& s) {
#pragma unroll
  for (int r = A; r < B; ++r) s[r] = __builtin_amdgcn_exp2f(s[r]);
}

DI void local_attn(char* __restrict__ ws, char* wl, int head, int tw, int l) {
  const char* qb = ws + WS_QA + (size_t)head * 5 * PSTR;
  const char* kb = qb + SZ_P;
  const char* vb = qb + 2 * SZ_P;
  const int token = tw + l;
  const int th = (tw - 16 + (l & 15)) < 0 ? 0 : tw - 16 + (l & 15);
  u32x2 qr[5], kr[5], vr[5], kh[5], vh[5];
#pragma unroll
  for (int i = 0; i < 5; ++i) {
    qr[i] = *reinterpret_cast<const u32x2*>(qb + (size_t)i * PSTR + token * 8);
    kr[i] = *reinterpret_cast<const u32x2*>(kb + (size_t)i * PSTR + token * 8);
    vr[i] = *reinterpret_cast<const u32x2*>(vb + (size_t)i * PSTR + token * 8);
    kh[i] = *reinterpret_cast<const u32x2*>(kb + (size_t)i * PSTR + th * 8);
    vh[i] = *reinterpret_cast<const u32x2*>(vb + (size_t)i * PSTR + th * 8);
  }
#pragma unroll
  for (int i = 0; i < 5; ++i) {
    *reinterpret_cast<u32x2*>(wl + i * 640 + (16 + l) * 8) = kr[i];
    *reinterpret_cast<u32x2*>(wl + 3200 + i * 640 + (16 + l) * 8) = vr[i];
    if (l < 16) {
      *reinterpret_cast<u32x2*>(wl + i * 640 + l * 8) = kh[i];
      *reinterpret_cast<u32x2*>(wl + 3200 + i * 640 + l * 8) = vh[i];
    }
  }
  WAITL0();
  __builtin_amdgcn_wave_barrier();
  float q[20];
#pragma unroll
  for (int i = 0; i < 5; ++i) { q[4 * i] = bf_lo(qr[i][0]); q[4 * i + 1] = bf_hi(qr[i][0]); q[4 * i + 2] = bf_lo(qr[i][1]); q[4 * i + 3] = bf_hi(qr[i][1]); }
  float lg[8];
#pragma unroll
  for (int wdx = 0; wdx < 8; ++wdx) {
    float a = 0.f;
#pragma unroll
    for (int i = 0; i < 5; ++i) {
      const u32x2 u = *reinterpret_cast<const u32x2*>(wl + i * 640 + (16 + l - 2 * wdx) * 8);
      a = fmaf(q[4 * i], bf_lo(u[0]), a); a = fmaf(q[4 * i + 1], bf_hi(u[0]), a);
      a = fmaf(q[4 * i + 2], bf_lo(u[1]), a); a = fmaf(q[4 * i + 3], bf_hi(u[1]), a);
    }
    a = rbf(rbf(a) * 0.22360679774997896f);
    lg[wdx] = (token - 2 * wdx < 0) ? rbf(-1e9f) : a;
  }
  float m = lg[0];
#pragma unroll
  for (int i = 1; i < 8; ++i) m = fmaxf(m, lg[i]);
  float sum = 0.f;
#pragma unroll
  for (int i = 0; i < 8; ++i) { lg[i] = __builtin_amdgcn_exp2f((lg[i] - m) * 1.4426950408889634f); sum += lg[i]; }
  const float inv = 1.0f / sum;
  float o[20];
#pragma unroll
  for (int i = 0; i < 20; ++i) o[i] = 0.f;
#pragma unroll
  for (int wdx = 0; wdx < 8; ++wdx) {
    const float pw = rbf(lg[wdx] * inv);
#pragma unroll
    for (int i = 0; i < 5; ++i) {
      const u32x2 u = *reinterpret_cast<const u32x2*>(wl + 3200 + i * 640 + (16 + l - 2 * wdx) * 8);
      o[4 * i] = fmaf(pw, bf_lo(u[0]), o[4 * i]); o[4 * i + 1] = fmaf(pw, bf_hi(u[0]), o[4 * i + 1]);
      o[4 * i + 2] = fmaf(pw, bf_lo(u[1]), o[4 * i + 2]); o[4 * i + 3] = fmaf(pw, bf_hi(u[1]), o[4 * i + 3]);
    }
  }
  char* ob = ws + WS_O + (size_t)head * 5 * PSTR + token * 8;
#pragma unroll
  for (int i = 0; i < 5; ++i) {
    u32x2 u = {pk2(o[4 * i], o[4 * i + 1]), pk2(o[4 * i + 2], o[4 * i + 3])};
    *reinterpret_cast<u32x2*>(ob + (size_t)i * PSTR) = u;
  }
}

DI void exact_attn(rsrc_t kv, unsigned koff, unsigned koff1, unsigned voff, const u32x4& q0, u32x4 q1, int h, f32x16& o) {
  if (h == 0) q1[2] = 0u;
  float m = -3.0e38f;
  o = zero16();
  KV f;
#pragma unroll 1
  for (int t = 0; t < 64; ++t) {
    load_k(f, kv, t, koff, koff1); load_v(f, kv, t, voff);
    f32x16 s = qk(f, q0, q1);
    float tm = s[0];
#pragma unroll
    for (int r = 1; r < 16; ++r) tm = fmaxf(tm, s[r]);
    tm = fmaxf(tm, __shfl_xor(tm, 32));
    const float mn = fmaxf(m, tm);
    const float alpha = __builtin_amdgcn_exp2f(m - mn);
    m = mn;
#pragma unroll
    for (int r = 0; r < 16; ++r) { o[r] *= alpha; s[r] = __builtin_amdgcn_exp2f(s[r] - mn); }
    o = MFMA32(f.v0, pack8(s, 0), o);
    o = MFMA32(f.v1, pack8(s, 1), o);
  }
}

DI float first_max(const f32x16& s) {
  float m = s[0];
#pragma unroll
  for (int r = 1; r < 16; ++r) m = fmaxf(m, s[r]);
  return rbf(fmaxf(m, __shfl_xor(m, 32)) + MAX_SLACK);
}

#define G4(S_, P_, i_) do { exps<4 * (i_), 4 * (i_) + 4>(S_); P_[2 * ((i_) & 1)] = pk2(S_[4 * (i_)], S_[4 * (i_) + 1]); P_[2 * ((i_) & 1) + 1] = pk2(S_[4 * (i_) + 2], S_[4 * (i_) + 3]); SB0(); } while (0)
#define K2_STEP(cur, nx, tld)                                                     \
  do {                                                                            \
    const int tb_ = (tld) & 63;                                                   \
    u32x4 p0_, p1_;                                                               \
    G4(sa, p0_, 0);                                                               \
    sb = MFMA32(BC8(cur.k0), BC8(qb0), zero16()); SB0();                          \
    G4(sa, p0_, 1);                                                               \
    sb = MFMA32(BC8(cur.k1), BC8(qb1), sb); SB0();                                \
    G4(sa, p1_, 2);                                                               \
    oa = MFMA32(cur.v0, BC8(p0_), oa); SB0();                                     \
    load_k(cur, kv, tb_, koff, koff1); SB0();                                           \
    G4(sa, p1_, 3);                                                               \
    oa = MFMA32(cur.v1, BC8(p1_), oa); SB0();                                     \
    G4(sb, p0_, 0);                                                               \
    sa = MFMA32(BC8(nx.k0), BC8(qa0), zero16()); SB0();                           \
    G4(sb, p0_, 1);                                                               \
    sa = MFMA32(BC8(nx.k1), BC8(qa1), sa); SB0();                                 \
    G4(sb, p1_, 2);                                                               \
    ob = MFMA32(cur.v0, BC8(p0_), ob); SB0();                                     \
    G4(sb, p1_, 3);                                                               \
    ob = MFMA32(cur.v1, BC8(p1_), ob); SB0();                                     \
    load_v(cur, kv, tb_, voff); SB0();                                            \
  } while (0)

__global__ __launch_bounds__(512) void k2_attn(char* __restrict__ ws, const u16* __restrict__ woA, const u16* __restrict__ woB) {
  extern __shared__ __attribute__((aligned(16))) char lds[];
  const int bid = blockIdx.x;
  const int head = (bid & 7) * 4 + ((bid >> 3) & 3), qt = bid >> 5;
  const int tid = threadIdx.x, l = tid & 63, r32 = l & 31, h = l >> 5;
  const int w = __builtin_amdgcn_readfirstlane(tid >> 6);
  const int qp = w & 3, kg = w >> 2;
  const rsrc_t kv = __builtin_amdgcn_make_buffer_rsrc(ws + WS_KVB + (size_t)head * 64 * KV_TILE, 0, 64 * KV_TILE, 0x00020000);
  const int t0 = qt * 256;
  const unsigned koff = h * 512 + r32 * 16, koff1 = 1024 + r32 * 16, voff = 1536 + h * 384 + (r32 < 20 ? r32 : 20) * 16;
  const int tstart = qt * 8 + (kg ? N0 : 0);
  const int nsteps = kg ? N1 : N0;

  unsigned pf_dummy = 0;
  if (w == 7 && l < 50) {
    const int g = bid * 50 + l;
    const char* p = (g < 6400 ? reinterpret_cast<const char*>(woA) : reinterpret_cast<const char*>(woB) - 6400 * 128) + (size_t)g * 128;
    asm volatile("global_load_dword %0, %1, off" : "=v"(pf_dummy) : "v"(p) : "memory");
  }

  KV f0, f1, f2, f3;
  u32x4 qa0, qa1, qb0, qb1;
  {
    const char* qp_ = ws + WS_QB + (size_t)head * 5 * PSTR + (t0 + qp * 64 + r32) * 8;
    const u32x2 a0 = *reinterpret_cast<const u32x2*>(qp_ + (size_t)(2 * h) * PSTR), b0 = *reinterpret_cast<const u32x2*>(qp_ + (size_t)(2 * h + 1) * PSTR);
    const u32x2 c0 = *reinterpret_cast<const u32x2*>(qp_ + (size_t)4 * PSTR);
    const u32x2 a1 = *reinterpret_cast<const u32x2*>(qp_ + 256 + (size_t)(2 * h) * PSTR), b1 = *reinterpret_cast<const u32x2*>(qp_ + 256 + (size_t)(2 * h + 1) * PSTR);
    const u32x2 c1 = *reinterpret_cast<const u32x2*>(qp_ + 256 + (size_t)4 * PSTR);
    qa0 = u32x4{a0[0], a0[1], b0[0], b0[1]}; qa1 = h ? u32x4{0u, 0u, 0u, 0u} : u32x4{c0[0], c0[1], 0u, 0u};
    qb0 = u32x4{a1[0], a1[1], b1[0], b1[1]}; qb1 = h ? u32x4{0u, 0u, 0u, 0u} : u32x4{c1[0], c1[1], 0u, 0u};
  }
  load_k(f0, kv, (tstart + 0) & 63, koff, koff1); load_v(f0, kv, (tstart + 0) & 63, voff);
  load_k(f1, kv, (tstart + 1) & 63, koff, koff1); load_v(f1, kv, (tstart + 1) & 63, voff);
  load_k(f2, kv, (tstart + 2) & 63, koff, koff1); load_v(f2, kv, (tstart + 2) & 63, voff);
  load_k(f3, kv, (tstart + 3) & 63, koff, koff1); load_v(f3, kv, (tstart + 3) & 63, voff);

  const float ma = first_max(qk(f0, qa0, qa1)), mb = first_max(qk(f0, qb0, qb1));
  if (h == 0) { qa1[2] = pk2(-ma, 0.f); qb1[2] = pk2(-mb, 0.f); }
  f32x16 oa = zero16(), ob = zero16();
  f32x16 sa = qk(f0, qa0, qa1), sb;

#pragma unroll 1
  for (int t = tstart; t < tstart + nsteps; t += 4) {
    K2_STEP(f0, f1, t + 4);
    K2_STEP(f1, f2, t + 5);
    K2_STEP(f2, f3, t + 6);
    K2_STEP(f3, f0, t + 7);
  }
  asm volatile("" :: "v"(pf_dummy));
  if (!kg) local_attn(ws, lds + K2_CMB + qp * 6400, head, t0 + qp * 64, l);

  float* xb = reinterpret_cast<float*>(lds) + qp * (2 * 13 * 64) + l;
  if (kg) {
#pragma unroll
    for (int r = 0; r < 12; ++r) { xb[r * 64] = oa[r]; xb[(13 + r) * 64] = ob[r]; }
    xb[12 * 64] = ma; xb[25 * 64] = mb;
  }
  __syncthreads();
  if (kg) return;
  {
    const float fa = __builtin_amdgcn_exp2f(xb[12 * 64] - ma), fb = __builtin_amdgcn_exp2f(xb[25 * 64] - mb);
#pragma unroll
    for (int r = 0; r < 12; ++r) { oa[r] = fmaf(xb[r * 64], fa, oa[r]); ob[r] = fmaf(xb[(13 + r) * 64], fb, ob[r]); }
  }
  float la = __shfl_xor(oa[8], 32), lb = __shfl_xor(ob[8], 32);
  if (h) { la = oa[8]; lb = ob[8]; }
  if (__any(!(la < L_LIMIT))) { exact_attn(kv, koff, koff1, voff, qa0, qa1, h, oa); la = __shfl_xor(oa[8], 32); if (h) la = oa[8]; }
  if (__any(!(lb < L_LIMIT))) { exact_attn(kv, koff, koff1, voff, qb0, qb1, h, ob); lb = __shfl_xor(ob[8], 32); if (h) lb = ob[8]; }
  const float ia = 1.0f / la, ib = 1.0f / lb;
  char* op = ws + WS_O + SZ_P + (size_t)(head * 5 + h) * PSTR + (t0 + qp * 64 + r32) * 8;
  { u32x2 u = {pk2(oa[0] * ia, oa[1] * ia), pk2(oa[2] * ia, oa[3] * ia)}; *reinterpret_cast<u32x2*>(op) = u; }
  { u32x2 u = {pk2(oa[4] * ia, oa[5] * ia), pk2(oa[6] * ia, oa[7] * ia)}; *reinterpret_cast<u32x2*>(op + (size_t)2 * PSTR) = u; }
  { u32x2 u = {pk2(ob[0] * ib, ob[1] * ib), pk2(ob[2] * ib, ob[3] * ib)}; *reinterpret_cast<u32x2*>(op + 256) = u; }
  { u32x2 u = {pk2(ob[4] * ib, ob[5] * ib), pk2(ob[6] * ib, ob[7] * ib)}; *reinterpret_cast<u32x2*>(op + 256 + (size_t)2 * PSTR) = u; }
  if (h == 0) {
    { u32x2 u = {pk2(oa[8] * ia, oa[9] * ia), pk2(oa[10] * ia, oa[11] * ia)}; *reinterpret_cast<u32x2*>(op + (size_t)4 * PSTR) = u; }
    { u32x2 u = {pk2(ob[8] * ib, ob[9] * ib), pk2(ob[10] * ib, ob[11] * ib)}; *reinterpret_cast<u32x2*>(op + 256 + (size_t)4 * PSTR) = u; }
  }
}

constexpr int K3_STAGE = 32768, K3_LDS = 5 * K3_STAGE;

DI void k3_issue(const char* a, const char* b, unsigned lbase, int lw, unsigned ao, const unsigned (&bo)[4]) {
#pragma unroll
  for (int p = 0; p < 4; ++p) dma16(a + (size_t)(lw * 4 + p) * PSTR, ao, lbase + (lw * 4 + p) * 1024);
#pragma unroll
  for (int p = 0; p < 4; ++p) dma16(b + (lw * 4 + p) * (4 * D * 2), bo[p], lbase + 16384 + (lw * 4 + p) * 1024);
}

__global__ __launch_bounds__(512) void k3_out(const char* __restrict__ ws, const u16* __restrict__ woA, const u16* __restrict__ woB,
                                              u16* __restrict__ out) {
  extern __shared__ __attribute__((aligned(16))) char lds[];
  const int bid = blockIdx.x;
  const int L = (bid & 7) * 20 + (bid >> 3);
  const int br = L / 80, r = L % 80, mt = r / 5, nt = r % 5;
  const int tid = threadIdx.x, l = tid & 63, r32 = l & 31, h = l >> 5;
  const int w = __builtin_amdgcn_readfirstlane(tid >> 6);
  if (w >= 4) {
    const char* A = ws + WS_O + br * SZ_P + mt * 1024;
    const char* B = reinterpret_cast<const char*>((br ? woB : woA) + nt * 128);
    unsigned bo[4];
#pragma unroll
    for (int e = 0; e < 4; ++e) bo[e] = (l >> 4) * (D * 2) + (((l & 15) ^ (((l >> 4) << 2) | e)) << 4);
    const unsigned ao = l * 16, lb = lds_addr(lds);
    const int lw = w - 4;
    int k2 = (0x8530 >> ((mt >> 2) * 4)) & 15;
#pragma unroll
    for (int sidx = 0; sidx < 4; ++sidx) {
      k3_issue(A + (size_t)k2 * (16 * PSTR), B + k2 * (64 * D * 2), lb + sidx * K3_STAGE, lw, ao, bo); k2 = (k2 == 9) ? 0 : k2 + 1;
    }
    int nxt = 4 * K3_STAGE;
#pragma unroll 1
    for (int it = 0; it < 6; ++it) {
      WAITV(24);
      BAR();
      k3_issue(A + (size_t)k2 * (16 * PSTR), B + k2 * (64 * D * 2), lb + nxt, lw, ao, bo); k2 = (k2 == 9) ? 0 : k2 + 1;
      nxt = (nxt == 4 * K3_STAGE) ? 0 : nxt + K3_STAGE;
    }
    WAITV(24); BAR();
    WAITV(16); BAR();
    WAITV(8); BAR();
    WAITV(0); BAR();
    BAR();
    return;
  }
  const int wm = w & 1, wn = w >> 1;
  f32x16 acc[2][2];
  g_compute<true, 1, 128, 5>(lds, acc, wm, wn, l);
  WAITL0();
  BAR();
  char* wl = lds + w * (64 * 144);
#pragma unroll
  for (int i = 0; i < 2; ++i)
#pragma unroll
    for (int j = 0; j < 2; ++j)
#pragma unroll
      for (int g = 0; g < 4; ++g) {
        u32x2 v = {pk2(acc[i][j][4 * g], acc[i][j][4 * g + 1]), pk2(acc[i][j][4 * g + 2], acc[i][j][4 * g + 3])};
        *reinterpret_cast<u32x2*>(wl + (i * 32 + r32) * 144 + (j * 32 + 8 * g + 4 * h) * 2) = v;
      }
  WAITL0();
  __builtin_amdgcn_wave_barrier();
#pragma unroll
  for (int i = 0; i < 8; ++i) {
    const int row = i * 8 + (l >> 3), ch = l & 7;
    const u32x4 v = *reinterpret_cast<const u32x4*>(wl + row * 144 + ch * 16);
    const int token = mt * 128 + wm * 64 + row;
    *reinterpret_cast<u32x4*>(out + (size_t)token * 1280 + br * 640 + nt * 128 + wn * 64 + ch * 8) = v;
  }
}

extern "C" void kernel_launch(void* const* d_in, const int* in_sizes, int n_in,
                              void* d_out, int out_size, void* d_ws, size_t ws_size,
                              hipStream_t stream) {
  const u16* xA = (const u16*)d_in[0];
  const u16* wA = (const u16*)d_in[1];
  const u16* woA = (const u16*)d_in[2];
  const u16* xB = (const u16*)d_in[3];
  const u16* wB = (const u16*)d_in[4];
  const u16* woB = (const u16*)d_in[5];
  char* ws = (char*)d_ws;
  (void)hipFuncSetAttribute((const void*)k1_qkv, hipFuncAttributeMaxDynamicSharedMemorySize, K1_LDS);
  (void)hipFuncSetAttribute((const void*)k3_out, hipFuncAttributeMaxDynamicSharedMemorySize, K3_LDS);
  k1_qkv<<<240, K1_THREADS, K1_LDS, stream>>>(xA, wA, xB, wB, ws);
  k2_attn<<<256, 512, K2_LDS, stream>>>(ws, woA, woB);
  k3_out<<<160, 512, K3_LDS, stream>>>(ws, woA, woB, (u16*)d_out);
}
```

```cpp
#include <hip/hip_runtime.h>
#include <hip/hip_bf16.h>

typedef __attribute__((ext_vector_type(8))) short bf16x8;
typedef __attribute__((ext_vector_type(4))) short s16x4;
typedef __attribute__((ext_vector_type(16))) float f32x16;
typedef __attribute__((ext_vector_type(2))) float f32x2;
typedef __attribute__((ext_vector_type(2))) __bf16 bf16x2;
typedef __attribute__((ext_vector_type(4))) unsigned u32x4;
typedef __attribute__((ext_vector_type(2))) unsigned u32x2;
typedef unsigned short u16;

#define DI __device__ __forceinline__
#define MFMA32(a, b, c) __builtin_amdgcn_mfma_f32_32x32x16_bf16((a), (b), (c), 0, 0, 0)
#define WAITV(n) asm volatile("s_waitcnt vmcnt(%0)" ::"n"(n) : "memory")
#define WAITL0() asm volatile("s_waitcnt lgkmcnt(0)" ::: "memory")
#define BAR() do { asm volatile("" ::: "memory"); __builtin_amdgcn_s_barrier(); asm volatile("" ::: "memory"); } while (0)
#define SB0() __builtin_amdgcn_sched_barrier(0)
#define BC8(x) __builtin_bit_cast(bf16x8, (x))

DI void dma16(const char* base, unsigned voff, unsigned ldsaddr) {
  unsigned keep;
  asm volatile("s_mov_b32 %0, m0\n\ts_mov_b32 m0, %3\n\ts_nop 0\n\tglobal_load_lds_dwordx4 %1, %2\n\ts_mov_b32 m0, %0"
               : "=&s"(keep) : "v"(voff), "s"(base), "s"(ldsaddr) : "memory");
}
DI unsigned lds_addr(const char* p) { return (unsigned)(size_t)((const __attribute__((address_space(3))) char*)p); }

constexpr int S = 2048, D = 640, N3 = 1920, H = 32;

constexpr int PSTR = S * 8 + 256;
constexpr size_t SZ_P = (size_t)160 * PSTR;
constexpr int KV_TILE = 3072;
constexpr size_t SZ_KV = (size_t)H * 64 * KV_TILE;
constexpr size_t WS_QB = 0;
constexpr size_t WS_KVB = WS_QB + SZ_P;
constexpr size_t WS_QA = WS_KVB + SZ_KV;
constexpr size_t WS_O = WS_QA + 3 * SZ_P;

DI unsigned pk2(float lo, float hi) {
  f32x2 v = {lo, hi};
  return __builtin_bit_cast(unsigned, __builtin_convertvector(v, bf16x2));
}
DI float bf_lo(unsigned u) { return __uint_as_float(u << 16); }
DI float bf_hi(unsigned u) { return __uint_as_float(u & 0xffff0000u); }
DI float rbf(float x) { return bf_lo(pk2(x, 0.f)); }

DI bf16x8 load_bfrag(const char* lb, int krow0, int col0, int l) {
  typedef __attribute__((address_space(3))) s16x4* lp4;
  const int h = l >> 5, i16 = l & 15, q = i16 >> 2, p = i16 & 3, blk = (l >> 4) & 1;
  const int c0 = col0 / 8 + blk * 2 + (p >> 1);
  const int k0 = krow0 + 8 * h + q, k1 = k0 + 4;
  const int f0 = ((k0 & 3) << 2) | ((k0 >> 2) & 3), f1 = ((k1 & 3) << 2) | ((k1 >> 2) & 3);
  const s16x4 lo = __builtin_amdgcn_ds_read_tr16_b64_v4i16((lp4)(lb + k0 * 256 + ((c0 ^ f0) << 4) + 8 * (p & 1)));
  const s16x4 hi = __builtin_amdgcn_ds_read_tr16_b64_v4i16((lp4)(lb + k1 * 256 + ((c0 ^ f1) << 4) + 8 * (p & 1)));
  return __builtin_shufflevector(lo, hi, 0, 1, 2, 3, 4, 5, 6, 7);
}

constexpr int K1_STAGE = 49152, K1_LDS = 3 * K1_STAGE, K1_THREADS = 768;
struct Frag1 { bf16x8 a[2], b[2]; };

template <int AMODE, int BM>
DI bf16x8 g_load_a(const char* la, int ks, int row, int h) {
  if constexpr (AMODE == 0) {
    return *reinterpret_cast<const bf16x8*>(la + row * 128 + (((2 * ks + h) ^ ((row >> 1) & 7)) << 4));
  } else {
    const s16x4 lo = *reinterpret_cast<const s16x4*>(la + ((4 * ks + 2 * h) * BM + row) * 8);
    const s16x4 hi = *reinterpret_cast<const s16x4*>(la + ((4 * ks + 2 * h + 1) * BM + row) * 8);
    return __builtin_shufflevector(lo, hi, 0, 1, 2, 3, 4, 5, 6, 7);
  }
}
#define GM(i, j) acc[i][j] = SWAP ? MFMA32(u.b[j], u.a[i], acc[i][j]) : MFMA32(u.a[i], u.b[j], acc[i][j])
template <bool SWAP>
DI void g_mma(const Frag1& u, f32x16 (&acc)[2][2]) { GM(0, 0); GM(0, 1); GM(1, 0); GM(1, 1); }
template <bool SWAP, int AMODE, int BM>
DI void g_step(const Frag1& u, f32x16 (&acc)[2][2], Frag1& f, const char* la, int ks, int wm, int wn, int l) {
  const int r32 = l & 31, h = l >> 5;
  GM(0, 0); SB0();
  f.a[0] = g_load_a<AMODE, BM>(la, ks, wm * 64 + r32, h); SB0();
  GM(0, 1); SB0();
  f.b[0] = load_bfrag(la + BM * 128, 16 * ks, wn * 64, l); SB0();
  GM(1, 0); SB0();
  f.a[1] = g_load_a<AMODE, BM>(la, ks, wm * 64 + 32 + r32, h); SB0();
  GM(1, 1); SB0();
  f.b[1] = load_bfrag(la + BM * 128, 16 * ks, wn * 64 + 32, l); SB0();
}
template <bool SWAP, int AMODE, int BM, int NSTAGE>
DI void g_compute(const char* lds, f32x16 (&acc)[2][2], int wm, int wn, int l) {
  constexpr int STAGE = BM * 128 + 16384;
#pragma unroll
  for (int i = 0; i < 2; ++i)
#pragma unroll
    for (int j = 0; j < 2; ++j)
#pragma unroll
      for (int r = 0; r < 16; ++r) acc[i][j][r] = 0.f;
  Frag1 f0, f1, f2, f3;
#pragma unroll
  for (int i = 0; i < 2; ++i) { f2.a[i] = f3.a[i] = bf16x8{0, 0, 0, 0, 0, 0, 0, 0}; f2.b[i] = f3.b[i] = bf16x8{0, 0, 0, 0, 0, 0, 0, 0}; }
  int cur = 0;
#pragma unroll 1
  for (int it = 0; it < 10; ++it) {
    WAITL0();
    BAR();
    const char* la = lds + cur;
    g_step<SWAP, AMODE, BM>(f2, acc, f0, la, 0, wm, wn, l);
    g_step<SWAP, AMODE, BM>(f3, acc, f1, la, 1, wm, wn, l);
    g_step<SWAP, AMODE, BM>(f0, acc, f2, la, 2, wm, wn, l);
    g_step<SWAP, AMODE, BM>(f1, acc, f3, la, 3, wm, wn, l);
    cur = (cur == (NSTAGE - 1) * STAGE) ? 0 : cur + STAGE;
  }
  g_mma<SWAP>(f2, acc);
  g_mma<SWAP>(f3, acc);
}

DI void k1_issue(const char* a, const char* b, unsigned lbase, int lw, const unsigned (&ao)[2], const unsigned (&bo)[4]) {
#pragma unroll
  for (int p = 0; p < 4; ++p) dma16(b + (lw * 4 + p) * (4 * N3 * 2), bo[p], lbase + 32768 + (lw * 4 + p) * 1024);
#pragma unroll
  for (int p = 0; p < 8; ++p) dma16(a + (lw * 8 + p) * 10240, ao[p & 1], lbase + (lw * 8 + p) * 1024);
}

__global__ __launch_bounds__(K1_THREADS) void k1_qkv(const u16* __restrict__ xA, const u16* __restrict__ wA,
                                              const u16* __restrict__ xB, const u16* __restrict__ wB, char* __restrict__ ws) {
  extern __shared__ __attribute__((aligned(16))) char lds[];
  const int bid = blockIdx.x;
  const int L = (bid & 7) * 30 + (bid >> 3);
  const int br = L / 120, r = L % 120, mt = r / 15, nt = r % 15;
  const int sec = nt / 5;
  const int tid = threadIdx.x, l = tid & 63, r32 = l & 31, h = l >> 5;
  const int w = __builtin_amdgcn_readfirstlane(tid >> 6);
  if (w >= 8) {
    const char* A = reinterpret_cast<const char*>((br ? xB : xA) + (size_t)mt * 256 * D);
    const char* B = reinterpret_cast<const char*>((br ? wB : wA) + nt * 128);
    unsigned ao[2], bo[4];
#pragma unroll
    for (int e = 0; e < 2; ++e) ao[e] = (l >> 3) * (D * 2) + (((l & 7) ^ (((l >> 4) + 4 * e) & 7)) << 4);
#pragma unroll
    for (int e = 0; e < 4; ++e) bo[e] = (l >> 4) * (N3 * 2) + (((l & 15) ^ (((l >> 4) << 2) | e)) << 4);
    const unsigned lb = lds_addr(lds);
    const int lw = w - 8;
    int k2 = (0x8530 >> ((mt >> 1) * 4)) & 15;
    k1_issue(A + k2 * 128, B + k2 * (64 * N3 * 2), lb, lw, ao, bo); k2 = (k2 == 9) ? 0 : k2 + 1;
    k1_issue(A + k2 * 128, B + k2 * (64 * N3 * 2), lb + K1_STAGE, lw, ao, bo); k2 = (k2 == 9) ? 0 : k2 + 1;
    int nxt = 2 * K1_STAGE;
#pragma unroll 1
    for (int it = 0; it < 8; ++it) {
      WAITV(12);
      BAR();
      k1_issue(A + k2 * 128, B + k2 * (64 * N3 * 2), lb + nxt, lw, ao, bo); k2 = (k2 == 9) ? 0 : k2 + 1;
      nxt = (nxt == 2 * K1_STAGE) ? 0 : nxt + K1_STAGE;
    }
    WAITV(12); BAR();
    WAITV(0); BAR();
    return;
  }
  const int wm = w & 3, wn = w >> 2;
  f32x16 acc[2][2];
  const bool vtype = (br == 1 && sec == 2);
  if (vtype) {
    g_compute<false, 0, 256, 3>(lds, acc, wm, wn, l);
#pragma unroll
    for (int j = 0; j < 2; ++j) {
      const int c = nt * 128 + wn * 64 + j * 32 + r32 - 1280;
      const int head = c / 20, d = c - head * 20;
#pragma unroll
      for (int i = 0; i < 2; ++i) {
        const int tile = mt * 8 + wm * 2 + i;
        char* base = ws + WS_KVB + ((size_t)head * 64 + tile) * KV_TILE + 1536 + h * 384 + d * 16;
#pragma unroll
        for (int s = 0; s < 2; ++s) {
          u32x4 v = {pk2(acc[i][j][8 * s], acc[i][j][8 * s + 1]), pk2(acc[i][j][8 * s + 2], acc[i][j][8 * s + 3]),
                     pk2(acc[i][j][8 * s + 4], acc[i][j][8 * s + 5]), pk2(acc[i][j][8 * s + 6], acc[i][j][8 * s + 7])};
          *reinterpret_cast<u32x4*>(base + s * 768) = v;
          if (d == 19) { u32x4 one = {0x3f803f80u, 0x3f803f80u, 0x3f803f80u, 0x3f803f80u}; *reinterpret_cast<u32x4*>(base + s * 768 + 16) = one; }
        }
      }
    }
  } else {
    g_compute<true, 0, 256, 3>(lds, acc, wm, wn, l);
    const bool isKB = (br == 1 && sec == 1), isQB = (br == 1 && sec == 0);
    const float qs = 0.22360679774997896f * 1.4426950408889634f;
    char* pbase = ws + (br ? WS_QB : WS_QA + sec * SZ_P);
#pragma unroll
    for (int i = 0; i < 2; ++i) {
      const int token = mt * 256 + wm * 64 + i * 32 + r32;
#pragma unroll
      for (int j = 0; j < 2; ++j) {
#pragma unroll
        for (int g = 0; g < 4; ++g) {
          const int c4 = (nt * 128 + wn * 64 + j * 32 + 8 * g + 4 * h - sec * 640) >> 2;
          unsigned lo = pk2(acc[i][j][4 * g], acc[i][j][4 * g + 1]), hi = pk2(acc[i][j][4 * g + 2], acc[i][j][4 * g + 3]);
          if (isKB) {
            const int head = c4 / 5, piece = c4 - head * 5;
            char* dst = ws + WS_KVB + ((size_t)head * 64 + (token >> 5)) * KV_TILE + (piece >> 1) * 512 + r32 * 16 + (piece & 1) * 8;
            if (piece == 4) { u32x4 v = {lo, hi, 0x00003f80u, 0u}; *reinterpret_cast<u32x4*>(dst) = v; }
            else { u32x2 v = {lo, hi}; *reinterpret_cast<u32x2*>(dst) = v; }
          } else {
            if (isQB) { lo = pk2(bf_lo(lo) * qs, bf_hi(lo) * qs); hi = pk2(bf_lo(hi) * qs, bf_hi(hi) * qs); }
            u32x2 v = {lo, hi}; *reinterpret_cast<u32x2*>(pbase + (size_t)c4 * PSTR + token * 8) = v;
          }
        }
      }
    }
  }
}

#define K2_N0 32
constexpr int N0 = K2_N0, N1 = 64 - K2_N0;
constexpr int K2_CMB = 4 * 2 * 13 * 256;
constexpr int K2_LDS = K2_CMB + 4 * 6400;
constexpr float MAX_SLACK = 16.f;
constexpr float L_LIMIT = 0.f;

DI bf16x8 pack8(const f32x16& x, int s) {
  u32x4 v = {pk2(x[8 * s], x[8 * s + 1]), pk2(x[8 * s + 2], x[8 * s + 3]), pk2(x[8 * s + 4], x[8 * s + 5]), pk2(x[8 * s + 6], x[8 * s + 7])};
  return BC8(v);
}
struct KV { u32x4 k0, k1; bf16x8 v0, v1; };
typedef __amdgpu_buffer_rsrc_t rsrc_t;
DI void load_k(KV& f, rsrc_t kv, int tile, unsigned koff, unsigned koff1) {
  f.k0 = __builtin_amdgcn_raw_buffer_load_b128(kv, koff, tile * KV_TILE, 0);
  f.k1 = __builtin_amdgcn_raw_buffer_load_b128(kv, koff1, tile * KV_TILE, 0);
}
DI void load_v(KV& f, rsrc_t kv, int tile, unsigned voff) {
  f.v0 = BC8(__builtin_amdgcn_raw_buffer_load_b128(kv, voff, tile * KV_TILE, 0));
  f.v1 = BC8(__builtin_amdgcn_raw_buffer_load_b128(kv, voff + 768, tile * KV_TILE, 0));
}
DI f32x16 zero16() { f32x16 z;
#pragma unroll
  for (int r = 0; r < 16; ++r) z[r] = 0.f;
  return z; }
DI f32x16 qk(const KV& k, const u32x4& q0, const u32x4& q1) {
  f32x16 s = MFMA32(BC8(k.k0), BC8(q0), zero16());
  return MFMA32(BC8(k.k1), BC8(q1), s);
}
template <int A, int B> DI void exps(f32x16& s) {
#pragma unroll
  for (int r = A; r < B; ++r) s[r] = __builtin_amdgcn_exp2f(s[r]);
}

DI void local_attn(char* __restrict__ ws, char* wl, int head, int tw, int l) {
  const char* qb = ws + WS_QA + (size_t)head * 5 * PSTR;
  const char* kb = qb + SZ_P;
  const char* vb = qb + 2 * SZ_P;
  const int token = tw + l;
  const int th = (tw - 16 + (l & 15)) < 0 ? 0 : tw - 16 + (l & 15);
  u32x2 qr[5], kr[5], vr[5], kh[5], vh[5];
#pragma unroll
  for (int i = 0; i < 5; ++i) {
    qr[i] = *reinterpret_cast<const u32x2*>(qb + (size_t)i * PSTR + token * 8);
    kr[i] = *reinterpret_cast<const u32x2*>(kb + (size_t)i * PSTR + token * 8);
    vr[i] = *reinterpret_cast<const u32x2*>(vb + (size_t)i * PSTR + token * 8);
    kh[i] = *reinterpret_cast<const u32x2*>(kb + (size_t)i * PSTR + th * 8);
    vh[i] = *reinterpret_cast<const u32x2*>(vb + (size_t)i * PSTR + th * 8);
  }
#pragma unroll
  for (int i = 0; i < 5; ++i) {
    *reinterpret_cast<u32x2*>(wl + i * 640 + (16 + l) * 8) = kr[i];
    *reinterpret_cast<u32x2*>(wl + 3200 + i * 640 + (16 + l) * 8) = vr[i];
    if (l < 16) {
      *reinterpret_cast<u32x2*>(wl + i * 640 + l * 8) = kh[i];
      *reinterpret_cast<u32x2*>(wl + 3200 + i * 640 + l * 8) = vh[i];
    }
  }
  WAITL0();
  __builtin_amdgcn_wave_barrier();
  float q[20];
#pragma unroll
  for (int i = 0; i < 5; ++i) { q[4 * i] = bf_lo(qr[i][0]); q[4 * i + 1] = bf_hi(qr[i][0]); q[4 * i + 2] = bf_lo(qr[i][1]); q[4 * i + 3] = bf_hi(qr[i][1]); }
  float lg[8];
#pragma unroll
  for (int wdx = 0; wdx < 8; ++wdx) {
    float a = 0.f;
#pragma unroll
    for (int i = 0; i < 5; ++i) {
      const u32x2 u = *reinterpret_cast<const u32x2*>(wl + i * 640 + (16 + l - 2 * wdx) * 8);
      a = fmaf(q[4 * i], bf_lo(u[0]), a); a = fmaf(q[4 * i + 1], bf_hi(u[0]), a);
      a = fmaf(q[4 * i + 2], bf_lo(u[1]), a); a = fmaf(q[4 * i + 3], bf_hi(u[1]), a);
    }
    a = rbf(rbf(a) * 0.22360679774997896f);
    lg[wdx] = (token - 2 * wdx < 0) ? rbf(-1e9f) : a;
  }
  float m = lg[0];
#pragma unroll
  for (int i = 1; i < 8; ++i) m = fmaxf(m, lg[i]);
  float sum = 0.f;
#pragma unroll
  for (int i = 0; i < 8; ++i) { lg[i] = __builtin_amdgcn_exp2f((lg[i] - m) * 1.4426950408889634f); sum += lg[i]; }
  const float inv = 1.0f / sum;
  float o[20];
#pragma unroll
  for (int i = 0; i < 20; ++i) o[i] = 0.f;
#pragma unroll
  for (int wdx = 0; wdx < 8; ++wdx) {
    const float pw = rbf(lg[wdx] * inv);
#pragma unroll
    for (int i = 0; i < 5; ++i) {
      const u32x2 u = *reinterpret_cast<const u32x2*>(wl + 3200 + i * 640 + (16 + l - 2 * wdx) * 8);
      o[4 * i] = fmaf(pw, bf_lo(u[0]), o[4 * i]); o[4 * i + 1] = fmaf(pw, bf_hi(u[0]), o[4 * i + 1]);
      o[4 * i + 2] = fmaf(pw, bf_lo(u[1]), o[4 * i + 2]); o[4 * i + 3] = fmaf(pw, bf_hi(u[1]), o[4 * i + 3]);
    }
  }
  char* ob = ws + WS_O + (size_t)head * 5 * PSTR + token * 8;
#pragma unroll
  for (int i = 0; i < 5; ++i) {
    u32x2 u = {pk2(o[4 * i], o[4 * i + 1]), pk2(o[4 * i + 2], o[4 * i + 3])};
    *reinterpret_cast<u32x2*>(ob + (size_t)i * PSTR) = u;
  }
}

DI void exact_attn(rsrc_t kv, unsigned koff, unsigned koff1, unsigned voff, const u32x4& q0, u32x4 q1, int h, f32x16& o) {
  if (h == 0) q1[2] = 0u;
  float m = -3.0e38f;
  o = zero16();
  KV f;
#pragma unroll 1
  for (int t = 0; t < 64; ++t) {
    load_k(f, kv, t, koff, koff1); load_v(f, kv, t, voff);
    f32x16 s = qk(f, q0, q1);
    float tm = s[0];
#pragma unroll
    for (int r = 1; r < 16; ++r) tm = fmaxf(tm, s[r]);
    tm = fmaxf(tm, __shfl_xor(tm, 32));
    const float mn = fmaxf(m, tm);
    const float alpha = __builtin_amdgcn_exp2f(m - mn);
    m = mn;
#pragma unroll
    for (int r = 0; r < 16; ++r) { o[r] *= alpha; s[r] = __builtin_amdgcn_exp2f(s[r] - mn); }
    o = MFMA32(f.v0, pack8(s, 0), o);
    o = MFMA32(f.v1, pack8(s, 1), o);
  }
}

DI float first_max(const f32x16& s) {
  float m = s[0];
#pragma unroll
  for (int r = 1; r < 16; ++r) m = fmaxf(m, s[r]);
  return rbf(fmaxf(m, __shfl_xor(m, 32)) + MAX_SLACK);
}

#define G4(S_, P_, i_) do { exps<4 * (i_), 4 * (i_) + 4>(S_); P_[2 * ((i_) & 1)] = pk2(S_[4 * (i_)], S_[4 * (i_) + 1]); P_[2 * ((i_) & 1) + 1] = pk2(S_[4 * (i_) + 2], S_[4 * (i_) + 3]); SB0(); } while (0)
#define K2_STEP(cur, nx, tld)                                                     \
  do {                                                                            \
    const int tb_ = (tld) & 63;                                                   \
    u32x4 p0_, p1_;                                                               \
    G4(sa, p0_, 0);                                                               \
    sb = MFMA32(BC8(cur.k0), BC8(qb0), zero16()); SB0();                          \
    G4(sa, p0_, 1);                                                               \
    sb = MFMA32(BC8(cur.k1), BC8(qb1), sb); SB0();                                \
    G4(sa, p1_, 2);                                                               \
    oa = MFMA32(cur.v0, BC8(p0_), oa); SB0();                                     \
    load_k(cur, kv, tb_, koff, koff1); SB0();                                           \
    G4(sa, p1_, 3);                                                               \
    oa = MFMA32(cur.v1, BC8(p1_), oa); SB0();                                     \
    G4(sb, p0_, 0);                                                               \
    sa = MFMA32(BC8(nx.k0), BC8(qa0), zero16()); SB0();                           \
    G4(sb, p0_, 1);                                                               \
    sa = MFMA32(BC8(nx.k1), BC8(qa1), sa); SB0();                                 \
    G4(sb, p1_, 2);                                                               \
    ob = MFMA32(cur.v0, BC8(p0_), ob); SB0();                                     \
    G4(sb, p1_, 3);                                                               \
    ob = MFMA32(cur.v1, BC8(p1_), ob); SB0();                                     \
    load_v(cur, kv, tb_, voff); SB0();                                            \
  } while (0)

__global__ __launch_bounds__(512) void k2_attn(char* __restrict__ ws, const u16* __restrict__ woA, const u16* __restrict__ woB) {
  extern __shared__ __attribute__((aligned(16))) char lds[];
  const int bid = blockIdx.x;
  const int head = (bid & 7) * 4 + ((bid >> 3) & 3), qt = bid >> 5;
  const int tid = threadIdx.x, l = tid & 63, r32 = l & 31, h = l >> 5;
  const int w = __builtin_amdgcn_readfirstlane(tid >> 6);
  const int qp = w & 3, kg = w >> 2;
  const rsrc_t kv = __builtin_amdgcn_make_buffer_rsrc(ws + WS_KVB + (size_t)head * 64 * KV_TILE, 0, 64 * KV_TILE, 0x00020000);
  const int t0 = qt * 256;
  const unsigned koff = h * 512 + r32 * 16, koff1 = 1024 + r32 * 16, voff = 1536 + h * 384 + (r32 < 20 ? r32 : 20) * 16;
  const int tstart = qt * 8 + (kg ? N0 : 0);
  const int nsteps = kg ? N1 : N0;

  unsigned pf_dummy = 0;
  if (w == 7 && l < 50) {
    const int g = bid * 50 + l;
    const char* p = (g < 6400 ? reinterpret_cast<const char*>(woA) : reinterpret_cast<const char*>(woB) - 6400 * 128) + (size_t)g * 128;
    asm volatile("global_load_dword %0, %1, off" : "=v"(pf_dummy) : "v"(p) : "memory");
  }

  KV f0, f1, f2, f3;
  u32x4 qa0, qa1, qb0, qb1;
  {
    const char* qp_ = ws + WS_QB + (size_t)head * 5 * PSTR + (t0 + qp * 64 + r32) * 8;
    const u32x2 a0 = *reinterpret_cast<const u32x2*>(qp_ + (size_t)(2 * h) * PSTR), b0 = *reinterpret_cast<const u32x2*>(qp_ + (size_t)(2 * h + 1) * PSTR);
    const u32x2 c0 = *reinterpret_cast<const u32x2*>(qp_ + (size_t)4 * PSTR);
    const u32x2 a1 = *reinterpret_cast<const u32x2*>(qp_ + 256 + (size_t)(2 * h) * PSTR), b1 = *reinterpret_cast<const u32x2*>(qp_ + 256 + (size_t)(2 * h + 1) * PSTR);
    const u32x2 c1 = *reinterpret_cast<const u32x2*>(qp_ + 256 + (size_t)4 * PSTR);
    qa0 = u32x4{a0[0], a0[1], b0[0], b0[1]}; qa1 = h ? u32x4{0u, 0u, 0u, 0u} : u32x4{c0[0], c0[1], 0u, 0u};
    qb0 = u32x4{a1[0], a1[1], b1[0], b1[1]}; qb1 = h ? u32x4{0u, 0u, 0u, 0u} : u32x4{c1[0], c1[1], 0u, 0u};
  }
  load_k(f0, kv, (tstart + 0) & 63, koff, koff1); load_v(f0, kv, (tstart + 0) & 63, voff);
  load_k(f1, kv, (tstart + 1) & 63, koff, koff1); load_v(f1, kv, (tstart + 1) & 63, voff);
  load_k(f2, kv, (tstart + 2) & 63, koff, koff1); load_v(f2, kv, (tstart + 2) & 63, voff);
  load_k(f3, kv, (tstart + 3) & 63, koff, koff1); load_v(f3, kv, (tstart + 3) & 63, voff);

  const float ma = first_max(qk(f0, qa0, qa1)), mb = first_max(qk(f0, qb0, qb1));
  if (h == 0) { qa1[2] = pk2(-ma, 0.f); qb1[2] = pk2(-mb, 0.f); }
  f32x16 oa = zero16(), ob = zero16();
  f32x16 sa = qk(f0, qa0, qa1), sb;

#pragma unroll 1
  for (int t = tstart; t < tstart + nsteps; t += 4) {
    K2_STEP(f0, f1, t + 4);
    K2_STEP(f1, f2, t + 5);
    K2_STEP(f2, f3, t + 6);
    K2_STEP(f3, f0, t + 7);
  }
  asm volatile("" :: "v"(pf_dummy));
#define K2_LOCAL_KG 0
  if (kg == K2_LOCAL_KG) local_attn(ws, lds + K2_CMB + qp * 6400, head, t0 + qp * 64, l);

  float* xb = reinterpret_cast<float*>(lds) + qp * (2 * 13 * 64) + l;
  if (kg) {
#pragma unroll
    for (int r = 0; r < 12; ++r) { xb[r * 64] = oa[r]; xb[(13 + r) * 64] = ob[r]; }
    xb[12 * 64] = ma; xb[25 * 64] = mb;
  }
  __syncthreads();
  if (kg) return;
  {
    const float fa = __builtin_amdgcn_exp2f(xb[12 * 64] - ma), fb = __builtin_amdgcn_exp2f(xb[25 * 64] - mb);
#pragma unroll
    for (int r = 0; r < 12; ++r) { oa[r] = fmaf(xb[r * 64], fa, oa[r]); ob[r] = fmaf(xb[(13 + r) * 64], fb, ob[r]); }
  }
  float la = __shfl_xor(oa[8], 32), lb = __shfl_xor(ob[8], 32);
  if (h) { la = oa[8]; lb = ob[8]; }
  if (__any(!(la < L_LIMIT))) { exact_attn(kv, koff, koff1, voff, qa0, qa1, h, oa); la = __shfl_xor(oa[8], 32); if (h) la = oa[8]; }
  if (__any(!(lb < L_LIMIT))) { exact_attn(kv, koff, koff1, voff, qb0, qb1, h, ob); lb = __shfl_xor(ob[8], 32); if (h) lb = ob[8]; }
  const float ia = 1.0f / la, ib = 1.0f / lb;
  char* op = ws + WS_O + SZ_P + (size_t)(head * 5 + h) * PSTR + (t0 + qp * 64 + r32) * 8;
  { u32x2 u = {pk2(oa[0] * ia, oa[1] * ia), pk2(oa[2] * ia, oa[3] * ia)}; *reinterpret_cast<u32x2*>(op) = u; }
  { u32x2 u = {pk2(oa[4] * ia, oa[5] * ia), pk2(oa[6] * ia, oa[7] * ia)}; *reinterpret_cast<u32x2*>(op + (size_t)2 * PSTR) = u; }
  { u32x2 u = {pk2(ob[0] * ib, ob[1] * ib), pk2(ob[2] * ib, ob[3] * ib)}; *reinterpret_cast<u32x2*>(op + 256) = u; }
  { u32x2 u = {pk2(ob[4] * ib, ob[5] * ib), pk2(ob[6] * ib, ob[7] * ib)}; *reinterpret_cast<u32x2*>(op + 256 + (size_t)2 * PSTR) = u; }
  if (h == 0) {
    { u32x2 u = {pk2(oa[8] * ia, oa[9] * ia), pk2(oa[10] * ia, oa[11] * ia)}; *reinterpret_cast<u32x2*>(op + (size_t)4 * PSTR) = u; }
    { u32x2 u = {pk2(ob[8] * ib, ob[9] * ib), pk2(ob[10] * ib, ob[11] * ib)}; *reinterpret_cast<u32x2*>(op + 256 + (size_t)4 * PSTR) = u; }
  }
}

constexpr int K3_STAGE = 32768, K3_LDS = 5 * K3_STAGE;

DI void k3_issue(const char* a, const char* b, unsigned lbase, int lw, unsigned ao, const unsigned (&bo)[4]) {
#pragma unroll
  for (int p = 0; p < 4; ++p) dma16(a + (size_t)(lw * 4 + p) * PSTR, ao, lbase + (lw * 4 + p) * 1024);
#pragma unroll
  for (int p = 0; p < 4; ++p) dma16(b + (lw * 4 + p) * (4 * D * 2), bo[p], lbase + 16384 + (lw * 4 + p) * 1024);
}

__global__ __launch_bounds__(512) void k3_out(const char* __restrict__ ws, const u16* __restrict__ woA, const u16* __restrict__ woB,
                                              u16* __restrict__ out) {
  extern __shared__ __attribute__((aligned(16))) char lds[];
  const int bid = blockIdx.x;
  const int L = (bid & 7) * 20 + (bid >> 3);
  const int br = L / 80, r = L % 80, mt = r / 5, nt = r % 5;
  const int tid = threadIdx.x, l = tid & 63, r32 = l & 31, h = l >> 5;
  const int w = __builtin_amdgcn_readfirstlane(tid >> 6);
  if (w >= 4) {
    const char* A = ws + WS_O + br * SZ_P + mt * 1024;
    const char* B = reinterpret_cast<const char*>((br ? woB : woA) + nt * 128);
    unsigned bo[4];
#pragma unroll
    for (int e = 0; e < 4; ++e) bo[e] = (l >> 4) * (D * 2) + (((l & 15) ^ (((l >> 4) << 2) | e)) << 4);
    const unsigned ao = l * 16, lb = lds_addr(lds);
    const int lw = w - 4;
    int k2 = (0x8530 >> ((mt >> 2) * 4)) & 15;
#pragma unroll
    for (int sidx = 0; sidx < 4; ++sidx) {
      k3_issue(A + (size_t)k2 * (16 * PSTR), B + k2 * (64 * D * 2), lb + sidx * K3_STAGE, lw, ao, bo); k2 = (k2 == 9) ? 0 : k2 + 1;
    }
    int nxt = 4 * K3_STAGE;
#pragma unroll 1
    for (int it = 0; it < 6; ++it) {
      WAITV(24);
      BAR();
      k3_issue(A + (size_t)k2 * (16 * PSTR), B + k2 * (64 * D * 2), lb + nxt, lw, ao, bo); k2 = (k2 == 9) ? 0 : k2 + 1;
      nxt = (nxt == 4 * K3_STAGE) ? 0 : nxt + K3_STAGE;
    }
    WAITV(24); BAR();
    WAITV(16); BAR();
    WAITV(8); BAR();
    WAITV(0); BAR();
    BAR();
    return;
  }
  const int wm = w & 1, wn = w >> 1;
  f32x16 acc[2][2];
  g_compute<true, 1, 128, 5>(lds, acc, wm, wn, l);
  WAITL0();
  BAR();
  char* wl = lds + w * (64 * 144);
#pragma unroll
  for (int i = 0; i < 2; ++i)
#pragma unroll
    for (int j = 0; j < 2; ++j)
#pragma unroll
      for (int g = 0; g < 4; ++g) {
        u32x2 v = {pk2(acc[i][j][4 * g], acc[i][j][4 * g + 1]), pk2(acc[i][j][4 * g + 2], acc[i][j][4 * g + 3])};
        *reinterpret_cast<u32x2*>(wl + (i * 32 + r32) * 144 + (j * 32 + 8 * g + 4 * h) * 2) = v;
      }
  WAITL0();
  __builtin_amdgcn_wave_barrier();
#pragma unroll
  for (int i = 0; i < 8; ++i) {
    const int row = i * 8 + (l >> 3), ch = l & 7;
    const u32x4 v = *reinterpret_cast<const u32x4*>(wl + row * 144 + ch * 16);
    const int token = mt * 128 + wm * 64 + row;
    *reinterpret_cast<u32x4*>(out + (size_t)token * 1280 + br * 640 + nt * 128 + wn * 64 + ch * 8) = v;
  }
}

extern "C" void kernel_launch(void* const* d_in, const int* in_sizes, int n_in,
                              void* d_out, int out_size, void* d_ws, size_t ws_size,
                              hipStream_t stream) {
  const u16* xA = (const u16*)d_in[0];
  const u16* wA = (const u16*)d_in[1];
  const u16* woA = (const u16*)d_in[2];
  const u16* xB = (const u16*)d_in[3];
  const u16* wB = (const u16*)d_in[4];
  const u16* woB = (const u16*)d_in[5];
  char* ws = (char*)d_ws;
  (void)hipFuncSetAttribute((const void*)k1_qkv, hipFuncAttributeMaxDynamicSharedMemorySize, K1_LDS);
  (void)hipFuncSetAttribute((const void*)k3_out, hipFuncAttributeMaxDynamicSharedMemorySize, K3_LDS);
  k1_qkv<<<240, K1_THREADS, K1_LDS, stream>>>(xA, wA, xB, wB, ws);
  k2_attn<<<256, 512, K2_LDS, stream>>>(ws, woA, woB);
  k3_out<<<160, 512, K3_LDS, stream>>>(ws, woA, woB, (u16*)d_out);
}
```

```cpp
#include <hip/hip_runtime.h>
#include <hip/hip_bf16.h>

typedef __attribute__((ext_vector_type(8))) short bf16x8;
typedef __attribute__((ext_vector_type(4))) short s16x4;
typedef __attribute__((ext_vector_type(16))) float f32x16;
typedef __attribute__((ext_vector_type(2))) float f32x2;
typedef __attribute__((ext_vector_type(2))) __bf16 bf16x2;
typedef __attribute__((ext_vector_type(4))) unsigned u32x4;
typedef __attribute__((ext_vector_type(2))) unsigned u32x2;
typedef unsigned short u16;

#define DI __device__ __forceinline__
#define MFMA32(a, b, c) __builtin_amdgcn_mfma_f32_32x32x16_bf16((a), (b), (c), 0, 0, 0)
#define WAITV(n) asm volatile("s_waitcnt vmcnt(%0)" ::"n"(n) : "memory")
#define WAITL0() asm volatile("s_waitcnt lgkmcnt(0)" ::: "memory")
#define BAR() do { asm volatile("" ::: "memory"); __builtin_amdgcn_s_barrier(); asm volatile("" ::: "memory"); } while (0)
#define SB0() __builtin_amdgcn_sched_barrier(0)
#define BC8(x) __builtin_bit_cast(bf16x8, (x))

DI void dma16(const char* base, unsigned voff, unsigned ldsaddr) {
  unsigned keep;
  asm volatile("s_mov_b32 %0, m0\n\ts_mov_b32 m0, %3\n\ts_nop 0\n\tglobal_load_lds_dwordx4 %1, %2\n\ts_mov_b32 m0, %0"
               : "=&s"(keep) : "v"(voff), "s"(base), "s"(ldsaddr) : "memory");
}
DI unsigned lds_addr(const char* p) { return (unsigned)(size_t)((const __attribute__((address_space(3))) char*)p); }

constexpr int S = 2048, D = 640, N3 = 1920, H = 32;

constexpr int PSTR = S * 8 + 256;
constexpr size_t SZ_P = (size_t)160 * PSTR;
constexpr int KV_TILE = 3072;
constexpr size_t SZ_KV = (size_t)H * 64 * KV_TILE;
constexpr size_t WS_QB = 0;
constexpr size_t WS_KVB = WS_QB + SZ_P;
constexpr size_t WS_QA = WS_KVB + SZ_KV;
constexpr size_t WS_O = WS_QA + 3 * SZ_P;

DI unsigned pk2(float lo, float hi) {
  f32x2 v = {lo, hi};
  return __builtin_bit_cast(unsigned, __builtin_convertvector(v, bf16x2));
}
DI float bf_lo(unsigned u) { return __uint_as_float(u << 16); }
DI float bf_hi(unsigned u) { return __uint_as_float(u & 0xffff0000u); }
DI float rbf(float x) { return bf_lo(pk2(x, 0.f)); }

DI bf16x8 load_bfrag(const char* lb, int krow0, int col0, int l) {
  typedef __attribute__((address_space(3))) s16x4* lp4;
  const int h = l >> 5, i16 = l & 15, q = i16 >> 2, p = i16 & 3, blk = (l >> 4) & 1;
  const int c0 = col0 / 8 + blk * 2 + (p >> 1);
  const int k0 = krow0 + 8 * h + q, k1 = k0 + 4;
  const int f0 = ((k0 & 3) << 2) | ((k0 >> 2) & 3), f1 = ((k1 & 3) << 2) | ((k1 >> 2) & 3);
  const s16x4 lo = __builtin_amdgcn_ds_read_tr16_b64_v4i16((lp4)(lb + k0 * 256 + ((c0 ^ f0) << 4) + 8 * (p & 1)));
  const s16x4 hi = __builtin_amdgcn_ds_read_tr16_b64_v4i16((lp4)(lb + k1 * 256 + ((c0 ^ f1) << 4) + 8 * (p & 1)));
  return __builtin_shufflevector(lo, hi, 0, 1, 2, 3, 4, 5, 6, 7);
}

constexpr int K1_STAGE = 49152, K1_LDS = 3 * K1_STAGE, K1_THREADS = 768;
struct Frag1 { bf16x8 a[2], b[2]; };

template <int AMODE, int BM>
DI bf16x8 g_load_a(const char* la, int ks, int row, int h) {
  if constexpr (AMODE == 0) {
    return *reinterpret_cast<const bf16x8*>(la + row * 128 + (((2 * ks + h) ^ ((row >> 1) & 7)) << 4));
  } else {
    const s16x4 lo = *reinterpret_cast<const s16x4*>(la + ((4 * ks + 2 * h) * BM + row) * 8);
    const s16x4 hi = *reinterpret_cast<const s16x4*>(la + ((4 * ks + 2 * h + 1) * BM + row) * 8);
    return __builtin_shufflevector(lo, hi, 0, 1, 2, 3, 4, 5, 6, 7);
  }
}
#define GM(i, j) acc[i][j] = SWAP ? MFMA32(u.b[j], u.a[i], acc[i][j]) : MFMA32(u.a[i], u.b[j], acc[i][j])
template <bool SWAP>
DI void g_mma(const Frag1& u, f32x16 (&acc)[2][2]) { GM(0, 0); GM(0, 1); GM(1, 0); GM(1, 1); }
template <bool SWAP, int AMODE, int BM>
DI void g_step(const Frag1& u, f32x16 (&acc)[2][2], Frag1& f, const char* la, int ks, int wm, int wn, int l) {
  const int r32 = l & 31, h = l >> 5;
  GM(0, 0); SB0();
  f.a[0] = g_load_a<AMODE, BM>(la, ks, wm * 64 + r32, h); SB0();
  GM(0, 1); SB0();
  f.b[0] = load_bfrag(la + BM * 128, 16 * ks, wn * 64, l); SB0();
  GM(1, 0); SB0();
  f.a[1] = g_load_a<AMODE, BM>(la, ks, wm * 64 + 32 + r32, h); SB0();
  GM(1, 1); SB0();
  f.b[1] = load_bfrag(la + BM * 128, 16 * ks, wn * 64 + 32, l); SB0();
}
template <bool SWAP, int AMODE, int BM, int NSTAGE>
DI void g_compute(const char* lds, f32x16 (&acc)[2][2], int wm, int wn, int l) {
  constexpr int STAGE = BM * 128 + 16384;
#pragma unroll
  for (int i = 0; i < 2; ++i)
#pragma unroll
    for (int j = 0; j < 2; ++j)
#pragma unroll
      for (int r = 0; r < 16; ++r) acc[i][j][r] = 0.f;
  Frag1 f0, f1, f2, f3;
#pragma unroll
  for (int i = 0; i < 2; ++i) { f2.a[i] = f3.a[i] = bf16x8{0, 0, 0, 0, 0, 0, 0, 0}; f2.b[i] = f3.b[i] = bf16x8{0, 0, 0, 0, 0, 0, 0, 0}; }
  int cur = 0;
#pragma unroll 1
  for (int it = 0; it < 10; ++it) {
    WAITL0();
    BAR();
    const char* la = lds + cur;
    g_step<SWAP, AMODE, BM>(f2, acc, f0, la, 0, wm, wn, l);
    g_step<SWAP, AMODE, BM>(f3, acc, f1, la, 1, wm, wn, l);
    g_step<SWAP, AMODE, BM>(f0, acc, f2, la, 2, wm, wn, l);
    g_step<SWAP, AMODE, BM>(f1, acc, f3, la, 3, wm, wn, l);
    cur = (cur == (NSTAGE - 1) * STAGE) ? 0 : cur + STAGE;
  }
  g_mma<SWAP>(f2, acc);
  g_mma<SWAP>(f3, acc);
}

DI void k1_issue(const char* a, const char* b, unsigned lbase, int lw, const unsigned (&ao)[2], const unsigned (&bo)[4]) {
#pragma unroll
  for (int p = 0; p < 4; ++p) dma16(b + (lw * 4 + p) * (4 * N3 * 2), bo[p], lbase + 32768 + (lw * 4 + p) * 1024);
#pragma unroll
  for (int p = 0; p < 8; ++p) dma16(a + (lw * 8 + p) * 10240, ao[p & 1], lbase + (lw * 8 + p) * 1024);
}

__global__ __launch_bounds__(K1_THREADS) void k1_qkv(const u16* __restrict__ xA, const u16* __restrict__ wA,
                                              const u16* __restrict__ xB, const u16* __restrict__ wB, char* __restrict__ ws) {
  extern __shared__ __attribute__((aligned(16))) char lds[];
  const int bid = blockIdx.x;
  const int L = (bid & 7) * 30 + (bid >> 3);
  const int br = L / 120, r = L % 120, mt = r / 15, nt = r % 15;
  const int sec = nt / 5;
  const int tid = threadIdx.x, l = tid & 63, r32 = l & 31, h = l >> 5;
  const int w = __builtin_amdgcn_readfirstlane(tid >> 6);
  if (w >= 8) {
    const char* A = reinterpret_cast<const char*>((br ? xB : xA) + (size_t)mt * 256 * D);
    const char* B = reinterpret_cast<const char*>((br ? wB : wA) + nt * 128);
    unsigned ao[2], bo[4];
#pragma unroll
    for (int e = 0; e < 2; ++e) ao[e] = (l >> 3) * (D * 2) + (((l & 7) ^ (((l >> 4) + 4 * e) & 7)) << 4);
#pragma unroll
    for (int e = 0; e < 4; ++e) bo[e] = (l >> 4) * (N3 * 2) + (((l & 15) ^ (((l >> 4) << 2) | e)) << 4);
    const unsigned lb = lds_addr(lds);
    const int lw = w - 8;
    int k2 = (0x8530 >> ((mt >> 1) * 4)) & 15;
    k1_issue(A + k2 * 128, B + k2 * (64 * N3 * 2), lb, lw, ao, bo); k2 = (k2 == 9) ? 0 : k2 + 1;
    k1_issue(A + k2 * 128, B + k2 * (64 * N3 * 2), lb + K1_STAGE, lw, ao, bo); k2 = (k2 == 9) ? 0 : k2 + 1;
    int nxt = 2 * K1_STAGE;
#pragma unroll 1
    for (int it = 0; it < 8; ++it) {
      WAITV(12);
      BAR();
      k1_issue(A + k2 * 128, B + k2 * (64 * N3 * 2), lb + nxt, lw, ao, bo); k2 = (k2 == 9) ? 0 : k2 + 1;
      nxt = (nxt == 2 * K1_STAGE) ? 0 : nxt + K1_STAGE;
    }
    WAITV(12); BAR();
    WAITV(0); BAR();
    return;
  }
  const int wm = w & 3, wn = w >> 2;
  f32x16 acc[2][2];
  const bool vtype = (br == 1 && sec == 2);
  if (vtype) {
    g_compute<false, 0, 256, 3>(lds, acc, wm, wn, l);
#pragma unroll
    for (int j = 0; j < 2; ++j) {
      const int c = nt * 128 + wn * 64 + j * 32 + r32 - 1280;
      const int head = c / 20, d = c - head * 20;
#pragma unroll
      for (int i = 0; i < 2; ++i) {
        const int tile = mt * 8 + wm * 2 + i;
        char* base = ws + WS_KVB + ((size_t)head * 64 + tile) * KV_TILE + 1536 + h * 384 + d * 16;
#pragma unroll
        for (int s = 0; s < 2; ++s) {
          u32x4 v = {pk2(acc[i][j][8 * s], acc[i][j][8 * s + 1]), pk2(acc[i][j][8 * s + 2], acc[i][j][8 * s + 3]),
                     pk2(acc[i][j][8 * s + 4], acc[i][j][8 * s + 5]), pk2(acc[i][j][8 * s + 6], acc[i][j][8 * s + 7])};
          *reinterpret_cast<u32x4*>(base + s * 768) = v;
          if (d == 19) { u32x4 one = {0x3f803f80u, 0x3f803f80u, 0x3f803f80u, 0x3f803f80u}; *reinterpret_cast<u32x4*>(base + s * 768 + 16) = one; }
        }
      }
    }
  } else {
    g_compute<true, 0, 256, 3>(lds, acc, wm, wn, l);
    const bool isKB = (br == 1 && sec == 1), isQB = (br == 1 && sec == 0);
    const float qs = 0.22360679774997896f * 1.4426950408889634f;
    char* pbase = ws + (br ? WS_QB : WS_QA + sec * SZ_P);
#pragma unroll
    for (int i = 0; i < 2; ++i) {
      const int token = mt * 256 + wm * 64 + i * 32 + r32;
#pragma unroll
      for (int j = 0; j < 2; ++j) {
#pragma unroll
        for (int g = 0; g < 4; ++g) {
          const int c4 = (nt * 128 + wn * 64 + j * 32 + 8 * g + 4 * h - sec * 640) >> 2;
          unsigned lo = pk2(acc[i][j][4 * g], acc[i][j][4 * g + 1]), hi = pk2(acc[i][j][4 * g + 2], acc[i][j][4 * g + 3]);
          if (isKB) {
            const int head = c4 / 5, piece = c4 - head * 5;
            char* dst = ws + WS_KVB + ((size_t)head * 64 + (token >> 5)) * KV_TILE + (piece >> 1) * 512 + r32 * 16 + (piece & 1) * 8;
            if (piece == 4) { u32x4 v = {lo, hi, 0x00003f80u, 0u}; *reinterpret_cast<u32x4*>(dst) = v; }
            else { u32x2 v = {lo, hi}; *reinterpret_cast<u32x2*>(dst) = v; }
          } else {
            if (isQB) { lo = pk2(bf_lo(lo) * qs, bf_hi(lo) * qs); hi = pk2(bf_lo(hi) * qs, bf_hi(hi) * qs); }
            u32x2 v = {lo, hi}; *reinterpret_cast<u32x2*>(pbase + (size_t)c4 * PSTR + token * 8) = v;
          }
        }
      }
    }
  }
}

#define K2_N0 32
constexpr int N0 = K2_N0, N1 = 64 - K2_N0;
constexpr int K2_CMB = 4 * 2 * 13 * 256;
constexpr int K2_LDS = K2_CMB + 4 * 6400;
constexpr float MAX_SLACK = 16.f;
constexpr float L_LIMIT = 281474976710656.f;

DI bf16x8 pack8(const f32x16& x, int s) {
  u32x4 v = {pk2(x[8 * s], x[8 * s + 1]), pk2(x[8 * s + 2], x[8 * s + 3]), pk2(x[8 * s + 4], x[8 * s + 5]), pk2(x[8 * s + 6], x[8 * s + 7])};
  return BC8(v);
}
struct KV { u32x4 k0, k1; bf16x8 v0, v1; };
typedef __amdgpu_buffer_rsrc_t rsrc_t;
DI void load_k(KV& f, rsrc_t kv, int tile, unsigned koff, unsigned koff1) {
  f.k0 = __builtin_amdgcn_raw_buffer_load_b128(kv, koff, tile * KV_TILE, 0);
  f.k1 = __builtin_amdgcn_raw_buffer_load_b128(kv, koff1, tile * KV_TILE, 0);
}
DI void load_v(KV& f, rsrc_t kv, int tile, unsigned voff) {
  f.v0 = BC8(__builtin_amdgcn_raw_buffer_load_b128(kv, voff, tile * KV_TILE, 0));
  f.v1 = BC8(__builtin_amdgcn_raw_buffer_load_b128(kv, voff + 768, tile * KV_TILE, 0));
}
DI f32x16 zero16() { f32x16 z;
#pragma unroll
  for (int r = 0; r < 16; ++r) z[r] = 0.f;
  return z; }
DI f32x16 qk(const KV& k, const u32x4& q0, const u32x4& q1) {
  f32x16 s = MFMA32(BC8(k.k0), BC8(q0), zero16());
  return MFMA32(BC8(k.k1), BC8(q1), s);
}
template <int A, int B> DI void exps(f32x16& s) {
#pragma unroll
  for (int r = A; r < B; ++r) s[r] = __builtin_amdgcn_exp2f(s[r]);
}

DI void local_attn(char* __restrict__ ws, char* wl, int head, int tw, int l) {
  const char* qb = ws + WS_QA + (size_t)head * 5 * PSTR;
  const char* kb = qb + SZ_P;
  const char* vb = qb + 2 * SZ_P;
  const int token = tw + l;
  const int th = (tw - 16 + (l & 15)) < 0 ? 0 : tw - 16 + (l & 15);
  u32x2 qr[5], kr[5], vr[5], kh[5], vh[5];
#pragma unroll
  for (int i = 0; i < 5; ++i) {
    qr[i] = *reinterpret_cast<const u32x2*>(qb + (size_t)i * PSTR + token * 8);
    kr[i] = *reinterpret_cast<const u32x2*>(kb + (size_t)i * PSTR + token * 8);
    vr[i] = *reinterpret_cast<const u32x2*>(vb + (size_t)i * PSTR + token * 8);
    kh[i] = *reinterpret_cast<const u32x2*>(kb + (size_t)i * PSTR + th * 8);
    vh[i] = *reinterpret_cast<const u32x2*>(vb + (size_t)i * PSTR + th * 8);
  }
#pragma unroll
  for (int i = 0; i < 5; ++i) {
    *reinterpret_cast<u32x2*>(wl + i * 640 + (16 + l) * 8) = kr[i];
    *reinterpret_cast<u32x2*>(wl + 3200 + i * 640 + (16 + l) * 8) = vr[i];
    if (l < 16) {
      *reinterpret_cast<u32x2*>(wl + i * 640 + l * 8) = kh[i];
      *reinterpret_cast<u32x2*>(wl + 3200 + i * 640 + l * 8) = vh[i];
    }
  }
  WAITL0();
  __builtin_amdgcn_wave_barrier();
  float q[20];
#pragma unroll
  for (int i = 0; i < 5; ++i) { q[4 * i] = bf_lo(qr[i][0]); q[4 * i + 1] = bf_hi(qr[i][0]); q[4 * i + 2] = bf_lo(qr[i][1]); q[4 * i + 3] = bf_hi(qr[i][1]); }
  float lg[8];
#pragma unroll
  for (int wdx = 0; wdx < 8; ++wdx) {
    float a = 0.f;
#pragma unroll
    for (int i = 0; i < 5; ++i) {
      const u32x2 u = *reinterpret_cast<const u32x2*>(wl + i * 640 + (16 + l - 2 * wdx) * 8);
      a = fmaf(q[4 * i], bf_lo(u[0]), a); a = fmaf(q[4 * i + 1], bf_hi(u[0]), a);
      a = fmaf(q[4 * i + 2], bf_lo(u[1]), a); a = fmaf(q[4 * i + 3], bf_hi(u[1]), a);
    }
    a = rbf(rbf(a) * 0.22360679774997896f);
    lg[wdx] = (token - 2 * wdx < 0) ? rbf(-1e9f) : a;
  }
  float m = lg[0];
#pragma unroll
  for (int i = 1; i < 8; ++i) m = fmaxf(m, lg[i]);
  float sum = 0.f;
#pragma unroll
  for (int i = 0; i < 8; ++i) { lg[i] = __builtin_amdgcn_exp2f((lg[i] - m) * 1.4426950408889634f); sum += lg[i]; }
  const float inv = 1.0f / sum;
  float o[20];
#pragma unroll
  for (int i = 0; i < 20; ++i) o[i] = 0.f;
#pragma unroll
  for (int wdx = 0; wdx < 8; ++wdx) {
    const float pw = rbf(lg[wdx] * inv);
#pragma unroll
    for (int i = 0; i < 5; ++i) {
      const u32x2 u = *reinterpret_cast<const u32x2*>(wl + 3200 + i * 640 + (16 + l - 2 * wdx) * 8);
      o[4 * i] = fmaf(pw, bf_lo(u[0]), o[4 * i]); o[4 * i + 1] = fmaf(pw, bf_hi(u[0]), o[4 * i + 1]);
      o[4 * i + 2] = fmaf(pw, bf_lo(u[1]), o[4 * i + 2]); o[4 * i + 3] = fmaf(pw, bf_hi(u[1]), o[4 * i + 3]);
    }
  }
  char* ob = ws + WS_O + (size_t)head * 5 * PSTR + token * 8;
#pragma unroll
  for (int i = 0; i < 5; ++i) {
    u32x2 u = {pk2(o[4 * i], o[4 * i + 1]), pk2(o[4 * i + 2], o[4 * i + 3])};
    *reinterpret_cast<u32x2*>(ob + (size_t)i * PSTR) = u;
  }
}

DI void exact_attn(rsrc_t kv, unsigned koff, unsigned koff1, unsigned voff, const u32x4& q0, u32x4 q1, int h, f32x16& o) {
  if (h == 0) q1[2] = 0u;
  float m = -3.0e38f;
  o = zero16();
  KV f;
#pragma unroll 1
  for (int t = 0; t < 64; ++t) {
    load_k(f, kv, t, koff, koff1); load_v(f, kv, t, voff);
    f32x16 s = qk(f, q0, q1);
    float tm = s[0];
#pragma unroll
    for (int r = 1; r < 16; ++r) tm = fmaxf(tm, s[r]);
    tm = fmaxf(tm, __shfl_xor(tm, 32));
    const float mn = fmaxf(m, tm);
    const float alpha = __builtin_amdgcn_exp2f(m - mn);
    m = mn;
#pragma unroll
    for (int r = 0; r < 16; ++r) { o[r] *= alpha; s[r] = __builtin_amdgcn_exp2f(s[r] - mn); }
    o = MFMA32(f.v0, pack8(s, 0), o);
    o = MFMA32(f.v1, pack8(s, 1), o);
  }
}

DI float first_max(const f32x16& s) {
  float m = s[0];
#pragma unroll
  for (int r = 1; r < 16; ++r) m = fmaxf(m, s[r]);
  return rbf(fmaxf(m, __shfl_xor(m, 32)) + MAX_SLACK);
}

#define G4(S_, P_, i_) do { exps<4 * (i_), 4 * (i_) + 4>(S_); P_[2 * ((i_) & 1)] = pk2(S_[4 * (i_)], S_[4 * (i_) + 1]); P_[2 * ((i_) & 1) + 1] = pk2(S_[4 * (i_) + 2], S_[4 * (i_) + 3]); SB0(); } while (0)
#define K2_STEP(cur, nx, tld)                                                     \
  do {                                                                            \
    const int tb_ = (tld) & 63;                                                   \
    u32x4 p0_, p1_;                                                               \
    G4(sa, p0_, 0);                                                               \
    sb = MFMA32(BC8(cur.k0), BC8(qb0), zero16()); SB0();                          \
    G4(sa, p0_, 1);                                                               \
    sb = MFMA32(BC8(cur.k1), BC8(qb1), sb); SB0();                                \
    G4(sa, p1_, 2);                                                               \
    oa = MFMA32(cur.v0, BC8(p0_), oa); SB0();                                     \
    load_k(cur, kv, tb_, koff, koff1); SB0();                                           \
    G4(sa, p1_, 3);                                                               \
    oa = MFMA32(cur.v1, BC8(p1_), oa); SB0();                                     \
    G4(sb, p0_, 0);                                                               \
    sa = MFMA32(BC8(nx.k0), BC8(qa0), zero16()); SB0();                           \
    G4(sb, p0_, 1);                                                               \
    sa = MFMA32(BC8(nx.k1), BC8(qa1), sa); SB0();                                 \
    G4(sb, p1_, 2);                                                               \
    ob = MFMA32(cur.v0, BC8(p0_), ob); SB0();                                     \
    G4(sb, p1_, 3);                                                               \
    ob = MFMA32(cur.v1, BC8(p1_), ob); SB0();                                     \
    load_v(cur, kv, tb_, voff); SB0();                                            \
  } while (0)

__global__ __launch_bounds__(512) void k2_attn(char* __restrict__ ws, const u16* __restrict__ woA, const u16* __restrict__ woB) {
  extern __shared__ __attribute__((aligned(16))) char lds[];
  const int bid = blockIdx.x;
  const int head = (bid & 7) * 4 + ((bid >> 3) & 3), qt = bid >> 5;
  const int tid = threadIdx.x, l = tid & 63, r32 = l & 31, h = l >> 5;
  const int w = __builtin_amdgcn_readfirstlane(tid >> 6);
  const int qp = w & 3, kg = w >> 2;
  const rsrc_t kv = __builtin_amdgcn_make_buffer_rsrc(ws + WS_KVB + (size_t)head * 64 * KV_TILE, 0, 64 * KV_TILE, 0x00020000);
  const int t0 = qt * 256;
  const unsigned koff = h * 512 + r32 * 16, koff1 = 1024 + r32 * 16, voff = 1536 + h * 384 + (r32 < 20 ? r32 : 20) * 16;
  const int tstart = qt * 8 + (kg ? N0 : 0);
  const int nsteps = kg ? N1 : N0;

  unsigned pf_dummy = 0;
  if (w == 7 && l < 50) {
    const int g = bid * 50 + l;
    const char* p = (g < 6400 ? reinterpret_cast<const char*>(woA) : reinterpret_cast<const char*>(woB) - 6400 * 128) + (size_t)g * 128;
    asm volatile("global_load_dword %0, %1, off" : "=v"(pf_dummy) : "v"(p) : "memory");
  }

  KV f0, f1, f2, f3;
  u32x4 qa0, qa1, qb0, qb1;
  {
    const char* qp_ = ws + WS_QB + (size_t)head * 5 * PSTR + (t0 + qp * 64 + r32) * 8;
    const u32x2 a0 = *reinterpret_cast<const u32x2*>(qp_ + (size_t)(2 * h) * PSTR), b0 = *reinterpret_cast<const u32x2*>(qp_ + (size_t)(2 * h + 1) * PSTR);
    const u32x2 c0 = *reinterpret_cast<const u32x2*>(qp_ + (size_t)4 * PSTR);
    const u32x2 a1 = *reinterpret_cast<const u32x2*>(qp_ + 256 + (size_t)(2 * h) * PSTR), b1 = *reinterpret_cast<const u32x2*>(qp_ + 256 + (size_t)(2 * h + 1) * PSTR);
    const u32x2 c1 = *reinterpret_cast<const u32x2*>(qp_ + 256 + (size_t)4 * PSTR);
    qa0 = u32x4{a0[0], a0[1], b0[0], b0[1]}; qa1 = h ? u32x4{0u, 0u, 0u, 0u} : u32x4{c0[0], c0[1], 0u, 0u};
    qb0 = u32x4{a1[0], a1[1], b1[0], b1[1]}; qb1 = h ? u32x4{0u, 0u, 0u, 0u} : u32x4{c1[0], c1[1], 0u, 0u};
  }
  load_k(f0, kv, (tstart + 0) & 63, koff, koff1); load_v(f0, kv, (tstart + 0) & 63, voff);
  load_k(f1, kv, (tstart + 1) & 63, koff, koff1); load_v(f1, kv, (tstart + 1) & 63, voff);
  load_k(f2, kv, (tstart + 2) & 63, koff, koff1); load_v(f2, kv, (tstart + 2) & 63, voff);
  load_k(f3, kv, (tstart + 3) & 63, koff, koff1); load_v(f3, kv, (tstart + 3) & 63, voff);

  const float ma = first_max(qk(f0, qa0, qa1)), mb = first_max(qk(f0, qb0, qb1));
  if (h == 0) { qa1[2] = pk2(-ma, 0.f); qb1[2] = pk2(-mb, 0.f); }
  f32x16 oa = zero16(), ob = zero16();
  f32x16 sa = qk(f0, qa0, qa1), sb;

#pragma unroll 1
  for (int t = tstart; t < tstart + nsteps; t += 4) {
    K2_STEP(f0, f1, t + 4);
    K2_STEP(f1, f2, t + 5);
    K2_STEP(f2, f3, t + 6);
    K2_STEP(f3, f0, t + 7);
  }
  asm volatile("" :: "v"(pf_dummy));
#define K2_LOCAL_KG 0
  if (kg == K2_LOCAL_KG) local_attn(ws, lds + K2_CMB + qp * 6400, head, t0 + qp * 64, l);

  float* xb = reinterpret_cast<float*>(lds) + qp * (2 * 13 * 64) + l;
  if (kg) {
#pragma unroll
    for (int r = 0; r < 12; ++r) { xb[r * 64] = oa[r]; xb[(13 + r) * 64] = ob[r]; }
    xb[12 * 64] = ma; xb[25 * 64] = mb;
  }
  __syncthreads();
  if (kg) return;
  bool bad_a = h && !(oa[8] < L_LIMIT && xb[8 * 64] < L_LIMIT), bad_b = h && !(ob[8] < L_LIMIT && xb[(13 + 8) * 64] < L_LIMIT);
  {
    const float fa = __builtin_amdgcn_exp2f(xb[12 * 64] - ma), fb = __builtin_amdgcn_exp2f(xb[25 * 64] - mb);
#pragma unroll
    for (int r = 0; r < 12; ++r) { oa[r] = fmaf(xb[r * 64], fa, oa[r]); ob[r] = fmaf(xb[(13 + r) * 64], fb, ob[r]); }
  }
  bad_a = bad_a || (h && !(oa[8] < L_LIMIT));
  bad_b = bad_b || (h && !(ob[8] < L_LIMIT));
  if (__any(bad_a)) exact_attn(kv, koff, koff1, voff, qa0, qa1, h, oa);
  if (__any(bad_b)) exact_attn(kv, koff, koff1, voff, qb0, qb1, h, ob);
  float la = __shfl_xor(oa[8], 32), lb = __shfl_xor(ob[8], 32);
  if (h) { la = oa[8]; lb = ob[8]; }
  const float ia = 1.0f / la, ib = 1.0f / lb;
  char* op = ws + WS_O + SZ_P + (size_t)(head * 5 + h) * PSTR + (t0 + qp * 64 + r32) * 8;
  { u32x2 u = {pk2(oa[0] * ia, oa[1] * ia), pk2(oa[2] * ia, oa[3] * ia)}; *reinterpret_cast<u32x2*>(op) = u; }
  { u32x2 u = {pk2(oa[4] * ia, oa[5] * ia), pk2(oa[6] * ia, oa[7] * ia)}; *reinterpret_cast<u32x2*>(op + (size_t)2 * PSTR) = u; }
  { u32x2 u = {pk2(ob[0] * ib, ob[1] * ib), pk2(ob[2] * ib, ob[3] * ib)}; *reinterpret_cast<u32x2*>(op + 256) = u; }
  { u32x2 u = {pk2(ob[4] * ib, ob[5] * ib), pk2(ob[6] * ib, ob[7] * ib)}; *reinterpret_cast<u32x2*>(op + 256 + (size_t)2 * PSTR) = u; }
  if (h == 0) {
    { u32x2 u = {pk2(oa[8] * ia, oa[9] * ia), pk2(oa[10] * ia, oa[11] * ia)}; *reinterpret_cast<u32x2*>(op + (size_t)4 * PSTR) = u; }
    { u32x2 u = {pk2(ob[8] * ib, ob[9] * ib), pk2(ob[10] * ib, ob[11] * ib)}; *reinterpret_cast<u32x2*>(op + 256 + (size_t)4 * PSTR) = u; }
  }
}

constexpr int K3_STAGE = 32768, K3_LDS = 5 * K3_STAGE;

DI void k3_issue(const char* a, const char* b, unsigned lbase, int lw, unsigned ao, const unsigned (&bo)[4]) {
#pragma unroll
  for (int p = 0; p < 4; ++p) dma16(a + (size_t)(lw * 4 + p) * PSTR, ao, lbase + (lw * 4 + p) * 1024);
#pragma unroll
  for (int p = 0; p < 4; ++p) dma16(b + (lw * 4 + p) * (4 * D * 2), bo[p], lbase + 16384 + (lw * 4 + p) * 1024);
}

__global__ __launch_bounds__(512) void k3_out(const char* __restrict__ ws, const u16* __restrict__ woA, const u16* __restrict__ woB,
                                              u16* __restrict__ out) {
  extern __shared__ __attribute__((aligned(16))) char lds[];
  const int bid = blockIdx.x;
  const int L = (bid & 7) * 20 + (bid >> 3);
  const int br = L / 80, r = L % 80, mt = r / 5, nt = r % 5;
  const int tid = threadIdx.x, l = tid & 63, r32 = l & 31, h = l >> 5;
  const int w = __builtin_amdgcn_readfirstlane(tid >> 6);
  if (w >= 4) {
    const char* A = ws + WS_O + br * SZ_P + mt * 1024;
    const char* B = reinterpret_cast<const char*>((br ? woB : woA) + nt * 128);
    unsigned bo[4];
#pragma unroll
    for (int e = 0; e < 4; ++e) bo[e] = (l >> 4) * (D * 2) + (((l & 15) ^ (((l >> 4) << 2) | e)) << 4);
    const unsigned ao = l * 16, lb = lds_addr(lds);
    const int lw = w - 4;
    int k2 = (0x8530 >> ((mt >> 2) * 4)) & 15;
#pragma unroll
    for (int sidx = 0; sidx < 4; ++sidx) {
      k3_issue(A + (size_t)k2 * (16 * PSTR), B + k2 * (64 * D * 2), lb + sidx * K3_STAGE, lw, ao, bo); k2 = (k2 == 9) ? 0 : k2 + 1;
    }
    int nxt = 4 * K3_STAGE;
#pragma unroll 1
    for (int it = 0; it < 6; ++it) {
      WAITV(24);
      BAR();
      k3_issue(A + (size_t)k2 * (16 * PSTR), B + k2 * (64 * D * 2), lb + nxt, lw, ao, bo); k2 = (k2 == 9) ? 0 : k2 + 1;
      nxt = (nxt == 4 * K3_STAGE) ? 0 : nxt + K3_STAGE;
    }
    WAITV(24); BAR();
    WAITV(16); BAR();
    WAITV(8); BAR();
    WAITV(0); BAR();
    BAR();
    return;
  }
  const int wm = w & 1, wn = w >> 1;
  f32x16 acc[2][2];
  g_compute<true, 1, 128, 5>(lds, acc, wm, wn, l);
  WAITL0();
  BAR();
  char* wl = lds + w * (64 * 144);
#pragma unroll
  for (int i = 0; i < 2; ++i)
#pragma unroll
    for (int j = 0; j < 2; ++j)
#pragma unroll
      for (int g = 0; g < 4; ++g) {
        u32x2 v = {pk2(acc[i][j][4 * g], acc[i][j][4 * g + 1]), pk2(acc[i][j][4 * g + 2], acc[i][j][4 * g + 3])};
        *reinterpret_cast<u32x2*>(wl + (i * 32 + r32) * 144 + (j * 32 + 8 * g + 4 * h) * 2) = v;
      }
  WAITL0();
  __builtin_amdgcn_wave_barrier();
#pragma unroll
  for (int i = 0; i < 8; ++i) {
    const int row = i * 8 + (l >> 3), ch = l & 7;
    const u32x4 v = *reinterpret_cast<const u32x4*>(wl + row * 144 + ch * 16);
    const int token = mt * 128 + wm * 64 + row;
    *reinterpret_cast<u32x4*>(out + (size_t)token * 1280 + br * 640 + nt * 128 + wn * 64 + ch * 8) = v;
  }
}

extern "C" void kernel_launch(void* const* d_in, const int* in_sizes, int n_in,
                              void* d_out, int out_size, void* d_ws, size_t ws_size,
                              hipStream_t stream) {
  const u16* xA = (const u16*)d_in[0];
  const u16* wA = (const u16*)d_in[1];
  const u16* woA = (const u16*)d_in[2];
  const u16* xB = (const u16*)d_in[3];
  const u16* wB = (const u16*)d_in[4];
  const u16* woB = (const u16*)d_in[5];
  char* ws = (char*)d_ws;
  (void)hipFuncSetAttribute((const void*)k1_qkv, hipFuncAttributeMaxDynamicSharedMemorySize, K1_LDS);
  (void)hipFuncSetAttribute((const void*)k3_out, hipFuncAttributeMaxDynamicSharedMemorySize, K3_LDS);
  k1_qkv<<<240, K1_THREADS, K1_LDS, stream>>>(xA, wA, xB, wB, ws);
  k2_attn<<<256, 512, K2_LDS, stream>>>(ws, woA, woB);
  k3_out<<<160, 512, K3_LDS, stream>>>(ws, woA, woB, (u16*)d_out);
}
```

```cpp
#include <hip/hip_runtime.h>
#include <hip/hip_bf16.h>

typedef __attribute__((ext_vector_type(8))) short bf16x8;
typedef __attribute__((ext_vector_type(4))) short s16x4;
typedef __attribute__((ext_vector_type(16))) float f32x16;
typedef __attribute__((ext_vector_type(2))) float f32x2;
typedef __attribute__((ext_vector_type(2))) __bf16 bf16x2;
typedef __attribute__((ext_vector_type(4))) unsigned u32x4;
typedef __attribute__((ext_vector_type(2))) unsigned u32x2;
typedef unsigned short u16;

#define DI __device__ __forceinline__
#define MFMA32(a, b, c) __builtin_amdgcn_mfma_f32_32x32x16_bf16((a), (b), (c), 0, 0, 0)
#define WAITV(n) asm volatile("s_waitcnt vmcnt(%0)" ::"n"(n) : "memory")
#define WAITL0() asm volatile("s_waitcnt lgkmcnt(0)" ::: "memory")
#define BAR() do { asm volatile("" ::: "memory"); __builtin_amdgcn_s_barrier(); asm volatile("" ::: "memory"); } while (0)
#define SB0() __builtin_amdgcn_sched_barrier(0)
#define BC8(x) __builtin_bit_cast(bf16x8, (x))

DI void dma16(const char* base, unsigned voff, unsigned ldsaddr) {
  unsigned keep;
  asm volatile("s_mov_b32 %0, m0\n\ts_mov_b32 m0, %3\n\ts_nop 0\n\tglobal_load_lds_dwordx4 %1, %2\n\ts_mov_b32 m0, %0"
               : "=&s"(keep) : "v"(voff), "s"(base), "s"(ldsaddr) : "memory");
}
DI unsigned lds_addr(const char* p) { return (unsigned)(size_t)((const __attribute__((address_space(3))) char*)p); }

constexpr int S = 2048, D = 640, N3 = 1920, H = 32;

constexpr int PSTR = S * 8 + 256;
constexpr size_t SZ_P = (size_t)160 * PSTR;
constexpr int KV_TILE = 3072;
constexpr size_t SZ_KV = (size_t)H * 64 * KV_TILE;
constexpr size_t WS_QB = 0;
constexpr size_t WS_KVB = WS_QB + SZ_P;
constexpr size_t WS_QA = WS_KVB + SZ_KV;
constexpr size_t WS_O = WS_QA + 3 * SZ_P;

DI unsigned pk2(float lo, float hi) {
  f32x2 v = {lo, hi};
  return __builtin_bit_cast(unsigned, __builtin_convertvector(v, bf16x2));
}
DI float bf_lo(unsigned u) { return __uint_as_float(u << 16); }
DI float bf_hi(unsigned u) { return __uint_as_float(u & 0xffff0000u); }
DI float rbf(float x) { return bf_lo(pk2(x, 0.f)); }

DI bf16x8 load_bfrag(const char* lb, int krow0, int col0, int l) {
  typedef __attribute__((address_space(3))) s16x4* lp4;
  const int h = l >> 5, i16 = l & 15, q = i16 >> 2, p = i16 & 3, blk = (l >> 4) & 1;
  const int c0 = col0 / 8 + blk * 2 + (p >> 1);
  const int k0 = krow0 + 8 * h + q, k1 = k0 + 4;
  const int f0 = ((k0 & 3) << 2) | ((k0 >> 2) & 3), f1 = ((k1 & 3) << 2) | ((k1 >> 2) & 3);
  const s16x4 lo = __builtin_amdgcn_ds_read_tr16_b64_v4i16((lp4)(lb + k0 * 256 + ((c0 ^ f0) << 4) + 8 * (p & 1)));
  const s16x4 hi = __builtin_amdgcn_ds_read_tr16_b64_v4i16((lp4)(lb + k1 * 256 + ((c0 ^ f1) << 4) + 8 * (p & 1)));
  return __builtin_shufflevector(lo, hi, 0, 1, 2, 3, 4, 5, 6, 7);
}

constexpr int K1_STAGE = 49152, K1_LDS = 3 * K1_STAGE, K1_THREADS = 768;
struct Frag1 { bf16x8 a[2], b[2]; };

template <int AMODE, int BM>
DI bf16x8 g_load_a(const char* la, int ks, int row, int h) {
  if constexpr (AMODE == 0) {
    return *reinterpret_cast<const bf16x8*>(la + row * 128 + (((2 * ks + h) ^ ((row >> 1) & 7)) << 4));
  } else {
    const s16x4 lo = *reinterpret_cast<const s16x4*>(la + ((4 * ks + 2 * h) * BM + row) * 8);
    const s16x4 hi = *reinterpret_cast<const s16x4*>(la + ((4 * ks + 2 * h + 1) * BM + row) * 8);
    return __builtin_shufflevector(lo, hi, 0, 1, 2, 3, 4, 5, 6, 7);
  }
}
#define GM(i, j) acc[i][j] = SWAP ? MFMA32(u.b[j], u.a[i], acc[i][j]) : MFMA32(u.a[i], u.b[j], acc[i][j])
template <bool SWAP>
DI void g_mma(const Frag1& u, f32x16 (&acc)[2][2]) { GM(0, 0); GM(0, 1); GM(1, 0); GM(1, 1); }
template <bool SWAP, int AMODE, int BM>
DI void g_step(const Frag1& u, f32x16 (&acc)[2][2], Frag1& f, const char* la, int ks, int wm, int wn, int l) {
  const int r32 = l & 31, h = l >> 5;
  GM(0, 0); SB0();
  f.a[0] = g_load_a<AMODE, BM>(la, ks, wm * 64 + r32, h); SB0();
  GM(0, 1); SB0();
  f.b[0] = load_bfrag(la + BM * 128, 16 * ks, wn * 64, l); SB0();
  GM(1, 0); SB0();
  f.a[1] = g_load_a<AMODE, BM>(la, ks, wm * 64 + 32 + r32, h); SB0();
  GM(1, 1); SB0();
  f.b[1] = load_bfrag(la + BM * 128, 16 * ks, wn * 64 + 32, l); SB0();
}
template <bool SWAP, int AMODE, int BM, int NSTAGE>
DI void g_compute(const char* lds, f32x16 (&acc)[2][2], int wm, int wn, int l) {
  constexpr int STAGE = BM * 128 + 16384;
#pragma unroll
  for (int i = 0; i < 2; ++i)
#pragma unroll
    for (int j = 0; j < 2; ++j)
#pragma unroll
      for (int r = 0; r < 16; ++r) acc[i][j][r] = 0.f;
  Frag1 f0, f1, f2, f3;
#pragma unroll
  for (int i = 0; i < 2; ++i) { f2.a[i] = f3.a[i] = bf16x8{0, 0, 0, 0, 0, 0, 0, 0}; f2.b[i] = f3.b[i] = bf16x8{0, 0, 0, 0, 0, 0, 0, 0}; }
  int cur = 0;
#pragma unroll 1
  for (int it = 0; it < 10; ++it) {
    WAITL0();
    BAR();

    const char* la = lds + cur;
    g_step<SWAP, AMODE, BM>(f2, acc, f0, la, 0, wm, wn, l);
    g_step<SWAP, AMODE, BM>(f3, acc, f1, la, 1, wm, wn, l);
    g_step<SWAP, AMODE, BM>(f0, acc, f2, la, 2, wm, wn, l);
    g_step<SWAP, AMODE, BM>(f1, acc, f3, la, 3, wm, wn, l);
    cur = (cur == (NSTAGE - 1) * STAGE) ? 0 : cur + STAGE;
  }
  g_mma<SWAP>(f2, acc);
  g_mma<SWAP>(f3, acc);
}

DI void k1_issue(const char* a, const char* b, unsigned lbase, int lw, const unsigned (&ao)[2], const unsigned (&bo)[4]) {
#pragma unroll
  for (int p = 0; p < 4; ++p) dma16(b + (lw * 4 + p) * (4 * N3 * 2), bo[p], lbase + 32768 + (lw * 4 + p) * 1024);
#pragma unroll
  for (int p = 0; p < 8; ++p) dma16(a + (lw * 8 + p) * 10240, ao[p & 1], lbase + (lw * 8 + p) * 1024);
}

__global__ __launch_bounds__(K1_THREADS) void k1_qkv(const u16* __restrict__ xA, const u16* __restrict__ wA,
                                              const u16* __restrict__ xB, const u16* __restrict__ wB, char* __restrict__ ws) {
  extern __shared__ __attribute__((aligned(16))) char lds[];
  const int bid = blockIdx.x;
  const int L = (bid & 7) * 30 + (bid >> 3);
  const int br = L / 120, r = L % 120;
  const int mh = r / 60, q60 = r % 60, nh = q60 >= 32;
  const int mt = mh * 4 + (nh ? (q60 - 32) / 7 : q60 / 8), nt = nh ? 8 + (q60 - 32) % 7 : q60 % 8;
  const int sec = nt / 5;
  const int tid = threadIdx.x, l = tid & 63, r32 = l & 31, h = l >> 5;
  const int w = __builtin_amdgcn_readfirstlane(tid >> 6);

  if (w >= 8) {
    const char* A = reinterpret_cast<const char*>((br ? xB : xA) + (size_t)mt * 256 * D);
    const char* B = reinterpret_cast<const char*>((br ? wB : wA) + nt * 128);
    unsigned ao[2], bo[4];
#pragma unroll
    for (int e = 0; e < 2; ++e) ao[e] = (l >> 3) * (D * 2) + (((l & 7) ^ (((l >> 4) + 4 * e) & 7)) << 4);
#pragma unroll
    for (int e = 0; e < 4; ++e) bo[e] = (l >> 4) * (N3 * 2) + (((l & 15) ^ (((l >> 4) << 2) | e)) << 4);
    const unsigned lb = lds_addr(lds);
    const int lw = w - 8;
    int k2 = 5 * (mh ^ nh);
    k1_issue(A + k2 * 128, B + k2 * (64 * N3 * 2), lb, lw, ao, bo); k2 = (k2 == 9) ? 0 : k2 + 1;
    k1_issue(A + k2 * 128, B + k2 * (64 * N3 * 2), lb + K1_STAGE, lw, ao, bo); k2 = (k2 == 9) ? 0 : k2 + 1;
    int nxt = 2 * K1_STAGE;
#pragma unroll 1
    for (int it = 0; it < 8; ++it) {
      WAITV(12);
      BAR();
      k1_issue(A + k2 * 128, B + k2 * (64 * N3 * 2), lb + nxt, lw, ao, bo); k2 = (k2 == 9) ? 0 : k2 + 1;
      nxt = (nxt == 2 * K1_STAGE) ? 0 : nxt + K1_STAGE;
    }
    WAITV(12); BAR();
    WAITV(0); BAR();
    return;
  }
  const int wm = w & 3, wn = w >> 2;
  f32x16 acc[2][2];
  const bool vtype = (br == 1 && sec == 2);
  if (vtype) {
    g_compute<false, 0, 256, 3>(lds, acc, wm, wn, l);

#pragma unroll
    for (int j = 0; j < 2; ++j) {
      const int c = nt * 128 + wn * 64 + j * 32 + r32 - 1280;
      const int head = c / 20, d = c - head * 20;
#pragma unroll
      for (int i = 0; i < 2; ++i) {
        const int tile = mt * 8 + wm * 2 + i;
        char* base = ws + WS_KVB + ((size_t)head * 64 + tile) * KV_TILE + 1536 + h * 384 + d * 16;
#pragma unroll
        for (int s = 0; s < 2; ++s) {
          u32x4 v = {pk2(acc[i][j][8 * s], acc[i][j][8 * s + 1]), pk2(acc[i][j][8 * s + 2], acc[i][j][8 * s + 3]),
                     pk2(acc[i][j][8 * s + 4], acc[i][j][8 * s + 5]), pk2(acc[i][j][8 * s + 6], acc[i][j][8 * s + 7])};
          *reinterpret_cast<u32x4*>(base + s * 768) = v;
          if (d == 19) { u32x4 one = {0x3f803f80u, 0x3f803f80u, 0x3f803f80u, 0x3f803f80u}; *reinterpret_cast<u32x4*>(base + s * 768 + 16) = one; }
        }
      }
    }
  } else {
    g_compute<true, 0, 256, 3>(lds, acc, wm, wn, l);

    const bool isKB = (br == 1 && sec == 1), isQB = (br == 1 && sec == 0);
    const float qs = 0.22360679774997896f * 1.4426950408889634f;
    char* pbase = ws + (br ? WS_QB : WS_QA + sec * SZ_P);
#pragma unroll
    for (int i = 0; i < 2; ++i) {
      const int token = mt * 256 + wm * 64 + i * 32 + r32;
#pragma unroll
      for (int j = 0; j < 2; ++j) {
#pragma unroll
        for (int g = 0; g < 4; ++g) {
          const int c4 = (nt * 128 + wn * 64 + j * 32 + 8 * g + 4 * h - sec * 640) >> 2;
          unsigned lo = pk2(acc[i][j][4 * g], acc[i][j][4 * g + 1]), hi = pk2(acc[i][j][4 * g + 2], acc[i][j][4 * g + 3]);
          if (isKB) {
            const int head = c4 / 5, piece = c4 - head * 5;
            char* dst = ws + WS_KVB + ((size_t)head * 64 + (token >> 5)) * KV_TILE + (piece >> 1) * 512 + r32 * 16 + (piece & 1) * 8;
            if (piece == 4) { u32x4 v = {lo, hi, 0x00003f80u, 0u}; *reinterpret_cast<u32x4*>(dst) = v; }
            else { u32x2 v = {lo, hi}; *reinterpret_cast<u32x2*>(dst) = v; }
          } else {
            if (isQB) { lo = pk2(bf_lo(lo) * qs, bf_hi(lo) * qs); hi = pk2(bf_lo(hi) * qs, bf_hi(hi) * qs); }
            u32x2 v = {lo, hi}; *reinterpret_cast<u32x2*>(pbase + (size_t)c4 * PSTR + token * 8) = v;
          }
        }
      }
    }
  }

}

#define K2_N0 32
constexpr int N0 = K2_N0, N1 = 64 - K2_N0;
constexpr int K2_CMB = 4 * 2 * 13 * 256;
constexpr int K2_LDS = K2_CMB + 4 * 6400;
constexpr float MAX_SLACK = 16.f;
constexpr float L_LIMIT = 281474976710656.f;

DI bf16x8 pack8(const f32x16& x, int s) {
  u32x4 v = {pk2(x[8 * s], x[8 * s + 1]), pk2(x[8 * s + 2], x[8 * s + 3]), pk2(x[8 * s + 4], x[8 * s + 5]), pk2(x[8 * s + 6], x[8 * s + 7])};
  return BC8(v);
}
struct KV { u32x4 k0, k1; bf16x8 v0, v1; };
typedef __amdgpu_buffer_rsrc_t rsrc_t;
DI void load_k(KV& f, rsrc_t kv, int tile, unsigned koff, unsigned koff1) {
  f.k0 = __builtin_amdgcn_raw_buffer_load_b128(kv, koff, tile * KV_TILE, 0);
  f.k1 = __builtin_amdgcn_raw_buffer_load_b128(kv, koff1, tile * KV_TILE, 0);
}
DI void load_v(KV& f, rsrc_t kv, int tile, unsigned voff) {
  f.v0 = BC8(__builtin_amdgcn_raw_buffer_load_b128(kv, voff, tile * KV_TILE, 0));
  f.v1 = BC8(__builtin_amdgcn_raw_buffer_load_b128(kv, voff + 768, tile * KV_TILE, 0));
}
DI f32x16 zero16() { f32x16 z;
#pragma unroll
  for (int r = 0; r < 16; ++r) z[r] = 0.f;
  return z; }
DI f32x16 qk(const KV& k, const u32x4& q0, const u32x4& q1) {
  f32x16 s = MFMA32(BC8(k.k0), BC8(q0), zero16());
  return MFMA32(BC8(k.k1), BC8(q1), s);
}
template <int A, int B> DI void exps(f32x16& s) {
#pragma unroll
  for (int r = A; r < B; ++r) s[r] = __builtin_amdgcn_exp2f(s[r]);
}

DI void local_attn(char* __restrict__ ws, char* wl, int head, int tw, int l) {
  const char* qb = ws + WS_QA + (size_t)head * 5 * PSTR;
  const char* kb = qb + SZ_P;
  const char* vb = qb + 2 * SZ_P;
  const int token = tw + l;
  const int th = (tw - 16 + (l & 15)) < 0 ? 0 : tw - 16 + (l & 15);
  u32x2 qr[5], kr[5], vr[5], kh[5], vh[5];
#pragma unroll
  for (int i = 0; i < 5; ++i) {
    qr[i] = *reinterpret_cast<const u32x2*>(qb + (size_t)i * PSTR + token * 8);
    kr[i] = *reinterpret_cast<const u32x2*>(kb + (size_t)i * PSTR + token * 8);
    vr[i] = *reinterpret_cast<const u32x2*>(vb + (size_t)i * PSTR + token * 8);
    kh[i] = *reinterpret_cast<const u32x2*>(kb + (size_t)i * PSTR + th * 8);
    vh[i] = *reinterpret_cast<const u32x2*>(vb + (size_t)i * PSTR + th * 8);
  }
#pragma unroll
  for (int i = 0; i < 5; ++i) {
    *reinterpret_cast<u32x2*>(wl + i * 640 + (16 + l) * 8) = kr[i];
    *reinterpret_cast<u32x2*>(wl + 3200 + i * 640 + (16 + l) * 8) = vr[i];
    if (l < 16) {
      *reinterpret_cast<u32x2*>(wl + i * 640 + l * 8) = kh[i];
      *reinterpret_cast<u32x2*>(wl + 3200 + i * 640 + l * 8) = vh[i];
    }
  }
  WAITL0();
  __builtin_amdgcn_wave_barrier();
  float q[20];
#pragma unroll
  for (int i = 0; i < 5; ++i) { q[4 * i] = bf_lo(qr[i][0]); q[4 * i + 1] = bf_hi(qr[i][0]); q[4 * i + 2] = bf_lo(qr[i][1]); q[4 * i + 3] = bf_hi(qr[i][1]); }
  float lg[8];
#pragma unroll
  for (int wdx = 0; wdx < 8; ++wdx) {
    float a = 0.f;
#pragma unroll
    for (int i = 0; i < 5; ++i) {
      const u32x2 u = *reinterpret_cast<const u32x2*>(wl + i * 640 + (16 + l - 2 * wdx) * 8);
      a = fmaf(q[4 * i], bf_lo(u[0]), a); a = fmaf(q[4 * i + 1], bf_hi(u[0]), a);
      a = fmaf(q[4 * i + 2], bf_lo(u[1]), a); a = fmaf(q[4 * i + 3], bf_hi(u[1]), a);
    }
    a = rbf(rbf(a) * 0.22360679774997896f);
    lg[wdx] = (token - 2 * wdx < 0) ? rbf(-1e9f) : a;
  }
  float m = lg[0];
#pragma unroll
  for (int i = 1; i < 8; ++i) m = fmaxf(m, lg[i]);
  float sum = 0.f;
#pragma unroll
  for (int i = 0; i < 8; ++i) { lg[i] = __builtin_amdgcn_exp2f((lg[i] - m) * 1.4426950408889634f); sum += lg[i]; }
  const float inv = 1.0f / sum;
  float o[20];
#pragma unroll
  for (int i = 0; i < 20; ++i) o[i] = 0.f;
#pragma unroll
  for (int wdx = 0; wdx < 8; ++wdx) {
    const float pw = rbf(lg[wdx] * inv);
#pragma unroll
    for (int i = 0; i < 5; ++i) {
      const u32x2 u = *reinterpret_cast<const u32x2*>(wl + 3200 + i * 640 + (16 + l - 2 * wdx) * 8);
      o[4 * i] = fmaf(pw, bf_lo(u[0]), o[4 * i]); o[4 * i + 1] = fmaf(pw, bf_hi(u[0]), o[4 * i + 1]);
      o[4 * i + 2] = fmaf(pw, bf_lo(u[1]), o[4 * i + 2]); o[4 * i + 3] = fmaf(pw, bf_hi(u[1]), o[4 * i + 3]);
    }
  }
  char* ob = ws + WS_O + (size_t)head * 5 * PSTR + token * 8;
#pragma unroll
  for (int i = 0; i < 5; ++i) {
    u32x2 u = {pk2(o[4 * i], o[4 * i + 1]), pk2(o[4 * i + 2], o[4 * i + 3])};
    *reinterpret_cast<u32x2*>(ob + (size_t)i * PSTR) = u;
  }
}

DI void exact_attn(rsrc_t kv, unsigned koff, unsigned koff1, unsigned voff, const u32x4& q0, u32x4 q1, int h, f32x16& o) {
  if (h == 0) q1[2] = 0u;
  float m = -3.0e38f;
  o = zero16();
  KV f;
#pragma unroll 1
  for (int t = 0; t < 64; ++t) {
    load_k(f, kv, t, koff, koff1); load_v(f, kv, t, voff);
    f32x16 s = qk(f, q0, q1);
    float tm = s[0];
#pragma unroll
    for (int r = 1; r < 16; ++r) tm = fmaxf(tm, s[r]);
    tm = fmaxf(tm, __shfl_xor(tm, 32));
    const float mn = fmaxf(m, tm);
    const float alpha = __builtin_amdgcn_exp2f(m - mn);
    m = mn;
#pragma unroll
    for (int r = 0; r < 16; ++r) { o[r] *= alpha; s[r] = __builtin_amdgcn_exp2f(s[r] - mn); }
    o = MFMA32(f.v0, pack8(s, 0), o);
    o = MFMA32(f.v1, pack8(s, 1), o);
  }
}

DI float first_max(const f32x16& s) {
  float m = s[0];
#pragma unroll
  for (int r = 1; r < 16; ++r) m = fmaxf(m, s[r]);
  return rbf(fmaxf(m, __shfl_xor(m, 32)) + MAX_SLACK);
}

#define G4(S_, P_, i_) do { exps<4 * (i_), 4 * (i_) + 4>(S_); P_[2 * ((i_) & 1)] = pk2(S_[4 * (i_)], S_[4 * (i_) + 1]); P_[2 * ((i_) & 1) + 1] = pk2(S_[4 * (i_) + 2], S_[4 * (i_) + 3]); SB0(); } while (0)
#define K2_STEP(cur, nx, tld)                                                     \
  do {                                                                            \
    const int tb_ = (tld) & 63;                                                   \
    u32x4 p0_, p1_;                                                               \
    G4(sa, p0_, 0);                                                               \
    sb = MFMA32(BC8(cur.k0), BC8(qb0), zero16()); SB0();                          \
    G4(sa, p0_, 1);                                                               \
    sb = MFMA32(BC8(cur.k1), BC8(qb1), sb); SB0();                                \
    G4(sa, p1_, 2);                                                               \
    oa = MFMA32(cur.v0, BC8(p0_), oa); SB0();                                     \
    load_k(cur, kv, tb_, koff, koff1); SB0();                                           \
    G4(sa, p1_, 3);                                                               \
    oa = MFMA32(cur.v1, BC8(p1_), oa); SB0();                                     \
    G4(sb, p0_, 0);                                                               \
    sa = MFMA32(BC8(nx.k0), BC8(qa0), zero16()); SB0();                           \
    G4(sb, p0_, 1);                                                               \
    sa = MFMA32(BC8(nx.k1), BC8(qa1), sa); SB0();                                 \
    G4(sb, p1_, 2);                                                               \
    ob = MFMA32(cur.v0, BC8(p0_), ob); SB0();                                     \
    G4(sb, p1_, 3);                                                               \
    ob = MFMA32(cur.v1, BC8(p1_), ob); SB0();                                     \
    load_v(cur, kv, tb_, voff); SB0();                                            \
  } while (0)

__global__ __launch_bounds__(512) void k2_attn(char* __restrict__ ws, const u16* __restrict__ woA, const u16* __restrict__ woB) {
  extern __shared__ __attribute__((aligned(16))) char lds[];
  const int bid = blockIdx.x;
  const int head = (bid & 7) * 4 + ((bid >> 3) & 3), qt = bid >> 5;
  const int tid = threadIdx.x, l = tid & 63, r32 = l & 31, h = l >> 5;
  const int w = __builtin_amdgcn_readfirstlane(tid >> 6);
  const int qp = w & 3, kg = w >> 2;
  const rsrc_t kv = __builtin_amdgcn_make_buffer_rsrc(ws + WS_KVB + (size_t)head * 64 * KV_TILE, 0, 64 * KV_TILE, 0x00020000);
  const int t0 = qt * 256;
  const unsigned koff = h * 512 + r32 * 16, koff1 = 1024 + r32 * 16, voff = 1536 + h * 384 + (r32 < 20 ? r32 : 20) * 16;

  const int tstart = qt * 8 + (kg ? N0 : 0);
  const int nsteps = kg ? N1 : N0;

  unsigned pf_dummy = 0;
  if (w == 7 && l < 50) {
    const int g = bid * 50 + l;
    const char* p = (g < 6400 ? reinterpret_cast<const char*>(woA) : reinterpret_cast<const char*>(woB) - 6400 * 128) + (size_t)g * 128;
    asm volatile("global_load_dword %0, %1, off" : "=v"(pf_dummy) : "v"(p) : "memory");
  }

  KV f0, f1, f2, f3;
  u32x4 qa0, qa1, qb0, qb1;
  {
    const char* qp_ = ws + WS_QB + (size_t)head * 5 * PSTR + (t0 + qp * 64 + r32) * 8;
    const u32x2 a0 = *reinterpret_cast<const u32x2*>(qp_ + (size_t)(2 * h) * PSTR), b0 = *reinterpret_cast<const u32x2*>(qp_ + (size_t)(2 * h + 1) * PSTR);
    const u32x2 c0 = *reinterpret_cast<const u32x2*>(qp_ + (size_t)4 * PSTR);
    const u32x2 a1 = *reinterpret_cast<const u32x2*>(qp_ + 256 + (size_t)(2 * h) * PSTR), b1 = *reinterpret_cast<const u32x2*>(qp_ + 256 + (size_t)(2 * h + 1) * PSTR);
    const u32x2 c1 = *reinterpret_cast<const u32x2*>(qp_ + 256 + (size_t)4 * PSTR);
    qa0 = u32x4{a0[0], a0[1], b0[0], b0[1]}; qa1 = h ? u32x4{0u, 0u, 0u, 0u} : u32x4{c0[0], c0[1], 0u, 0u};
    qb0 = u32x4{a1[0], a1[1], b1[0], b1[1]}; qb1 = h ? u32x4{0u, 0u, 0u, 0u} : u32x4{c1[0], c1[1], 0u, 0u};
  }
  load_k(f0, kv, (tstart + 0) & 63, koff, koff1);
  SB0();
  load_v(f0, kv, (tstart + 0) & 63, voff);
  load_k(f1, kv, (tstart + 1) & 63, koff, koff1); load_v(f1, kv, (tstart + 1) & 63, voff);

  const float ma = first_max(qk(f0, qa0, qa1)), mb = first_max(qk(f0, qb0, qb1));
  SB0();
  load_k(f2, kv, (tstart + 2) & 63, koff, koff1); load_v(f2, kv, (tstart + 2) & 63, voff);
  load_k(f3, kv, (tstart + 3) & 63, koff, koff1); load_v(f3, kv, (tstart + 3) & 63, voff);
  if (h == 0) { qa1[2] = pk2(-ma, 0.f); qb1[2] = pk2(-mb, 0.f); }
  f32x16 oa = zero16(), ob = zero16();
  f32x16 sa = qk(f0, qa0, qa1), sb;

#pragma unroll 1
  for (int t = tstart; t < tstart + nsteps; t += 4) {
    K2_STEP(f0, f1, t + 4);
    K2_STEP(f1, f2, t + 5);
    K2_STEP(f2, f3, t + 6);
    K2_STEP(f3, f0, t + 7);
  }

  asm volatile("" :: "v"(pf_dummy));
#define K2_LOCAL_KG 0
  if (kg == K2_LOCAL_KG) local_attn(ws, lds + K2_CMB + qp * 6400, head, t0 + qp * 64, l);

  float* xb = reinterpret_cast<float*>(lds) + qp * (2 * 13 * 64) + l;
  if (kg) {
#pragma unroll
    for (int r = 0; r < 12; ++r) { xb[r * 64] = oa[r]; xb[(13 + r) * 64] = ob[r]; }
    xb[12 * 64] = ma; xb[25 * 64] = mb;
  }
  __syncthreads();
  if (kg) return;
  bool bad_a = h && !(oa[8] < L_LIMIT && xb[8 * 64] < L_LIMIT), bad_b = h && !(ob[8] < L_LIMIT && xb[(13 + 8) * 64] < L_LIMIT);
  {
    const float fa = __builtin_amdgcn_exp2f(xb[12 * 64] - ma), fb = __builtin_amdgcn_exp2f(xb[25 * 64] - mb);
#pragma unroll
    for (int r = 0; r < 12; ++r) { oa[r] = fmaf(xb[r * 64], fa, oa[r]); ob[r] = fmaf(xb[(13 + r) * 64], fb, ob[r]); }
  }
  bad_a = bad_a || (h && !(oa[8] < L_LIMIT));
  bad_b = bad_b || (h && !(ob[8] < L_LIMIT));
  if (__any(bad_a)) exact_attn(kv, koff, koff1, voff, qa0, qa1, h, oa);
  if (__any(bad_b)) exact_attn(kv, koff, koff1, voff, qb0, qb1, h, ob);
  float la = __shfl_xor(oa[8], 32), lb = __shfl_xor(ob[8], 32);
  if (h) { la = oa[8]; lb = ob[8]; }
  const float ia = 1.0f / la, ib = 1.0f / lb;
  char* op = ws + WS_O + SZ_P + (size_t)(head * 5 + h) * PSTR + (t0 + qp * 64 + r32) * 8;
  { u32x2 u = {pk2(oa[0] * ia, oa[1] * ia), pk2(oa[2] * ia, oa[3] * ia)}; *reinterpret_cast<u32x2*>(op) = u; }
  { u32x2 u = {pk2(oa[4] * ia, oa[5] * ia), pk2(oa[6] * ia, oa[7] * ia)}; *reinterpret_cast<u32x2*>(op + (size_t)2 * PSTR) = u; }
  { u32x2 u = {pk2(ob[0] * ib, ob[1] * ib), pk2(ob[2] * ib, ob[3] * ib)}; *reinterpret_cast<u32x2*>(op + 256) = u; }
  { u32x2 u = {pk2(ob[4] * ib, ob[5] * ib), pk2(ob[6] * ib, ob[7] * ib)}; *reinterpret_cast<u32x2*>(op + 256 + (size_t)2 * PSTR) = u; }
  if (h == 0) {
    { u32x2 u = {pk2(oa[8] * ia, oa[9] * ia), pk2(oa[10] * ia, oa[11] * ia)}; *reinterpret_cast<u32x2*>(op + (size_t)4 * PSTR) = u; }
    { u32x2 u = {pk2(ob[8] * ib, ob[9] * ib), pk2(ob[10] * ib, ob[11] * ib)}; *reinterpret_cast<u32x2*>(op + 256 + (size_t)4 * PSTR) = u; }
  }

}

constexpr int K3_STAGE = 32768, K3_LDS = 5 * K3_STAGE;

DI void k3_issue(const char* a, const char* b, unsigned lbase, int lw, unsigned ao, const unsigned (&bo)[4]) {
#pragma unroll
  for (int p = 0; p < 4; ++p) dma16(a + (size_t)(lw * 4 + p) * PSTR, ao, lbase + (lw * 4 + p) * 1024);
#pragma unroll
  for (int p = 0; p < 4; ++p) dma16(b + (lw * 4 + p) * (4 * D * 2), bo[p], lbase + 16384 + (lw * 4 + p) * 1024);
}

__global__ __launch_bounds__(512) void k3_out(const char* __restrict__ ws, const u16* __restrict__ woA, const u16* __restrict__ woB,
                                              u16* __restrict__ out) {
  extern __shared__ __attribute__((aligned(16))) char lds[];
  const int bid = blockIdx.x;
  const int L = (bid & 7) * 20 + (bid >> 3);
  const int br = L / 80, r = L % 80, mt = r / 5, nt = r % 5;
  const int tid = threadIdx.x, l = tid & 63, r32 = l & 31, h = l >> 5;
  const int w = __builtin_amdgcn_readfirstlane(tid >> 6);

  if (w >= 4) {
    const char* A = ws + WS_O + br * SZ_P + mt * 1024;
    const char* B = reinterpret_cast<const char*>((br ? woB : woA) + nt * 128);
    unsigned bo[4];
#pragma unroll
    for (int e = 0; e < 4; ++e) bo[e] = (l >> 4) * (D * 2) + (((l & 15) ^ (((l >> 4) << 2) | e)) << 4);
    const unsigned ao = l * 16, lb = lds_addr(lds);
    const int lw = w - 4;
    int k2 = (0x8530 >> ((mt >> 2) * 4)) & 15;
#pragma unroll
    for (int sidx = 0; sidx < 4; ++sidx) {
      k3_issue(A + (size_t)k2 * (16 * PSTR), B + k2 * (64 * D * 2), lb + sidx * K3_STAGE, lw, ao, bo); k2 = (k2 == 9) ? 0 : k2 + 1;
    }
    int nxt = 4 * K3_STAGE;
#pragma unroll 1
    for (int it = 0; it < 6; ++it) {
      WAITV(24);
      BAR();
      k3_issue(A + (size_t)k2 * (16 * PSTR), B + k2 * (64 * D * 2), lb + nxt, lw, ao, bo); k2 = (k2 == 9) ? 0 : k2 + 1;
      nxt = (nxt == 4 * K3_STAGE) ? 0 : nxt + K3_STAGE;
    }
    WAITV(24); BAR();
    WAITV(16); BAR();
    WAITV(8); BAR();
    WAITV(0); BAR();
    BAR();
    return;
  }
  const int wm = w & 1, wn = w >> 1;
  f32x16 acc[2][2];
  g_compute<true, 1, 128, 5>(lds, acc, wm, wn, l);

  WAITL0();
  BAR();
  char* wl = lds + w * (64 * 144);
#pragma unroll
  for (int i = 0; i < 2; ++i)
#pragma unroll
    for (int j = 0; j < 2; ++j)
#pragma unroll
      for (int g = 0; g < 4; ++g) {
        u32x2 v = {pk2(acc[i][j][4 * g], acc[i][j][4 * g + 1]), pk2(acc[i][j][4 * g + 2], acc[i][j][4 * g + 3])};
        *reinterpret_cast<u32x2*>(wl + (i * 32 + r32) * 144 + (j * 32 + 8 * g + 4 * h) * 2) = v;
      }
  WAITL0();
  __builtin_amdgcn_wave_barrier();
#pragma unroll
  for (int i = 0; i < 8; ++i) {
    const int row = i * 8 + (l >> 3), ch = l & 7;
    const u32x4 v = *reinterpret_cast<const u32x4*>(wl + row * 144 + ch * 16);
    const int token = mt * 128 + wm * 64 + row;
    *reinterpret_cast<u32x4*>(out + (size_t)token * 1280 + br * 640 + nt * 128 + wn * 64 + ch * 8) = v;
  }

}

extern "C" void kernel_launch(void* const* d_in, const int* in_sizes, int n_in,
                              void* d_out, int out_size, void* d_ws, size_t ws_size,
                              hipStream_t stream) {
  const u16* xA = (const u16*)d_in[0];
  const u16* wA = (const u16*)d_in[1];
  const u16* woA = (const u16*)d_in[2];
  const u16* xB = (const u16*)d_in[3];
  const u16* wB = (const u16*)d_in[4];
  const u16* woB = (const u16*)d_in[5];
  char* ws = (char*)d_ws;
  (void)hipFuncSetAttribute((const void*)k1_qkv, hipFuncAttributeMaxDynamicSharedMemorySize, K1_LDS);
  (void)hipFuncSetAttribute((const void*)k3_out, hipFuncAttributeMaxDynamicSharedMemorySize, K3_LDS);
  k1_qkv<<<240, K1_THREADS, K1_LDS, stream>>>(xA, wA, xB, wB, ws);
  k2_attn<<<256, 512, K2_LDS, stream>>>(ws, woA, woB);
  k3_out<<<160, 512, K3_LDS, stream>>>(ws, woA, woB, (u16*)d_out);
}
```
